```python
import torch

def _softmax(x, axis=-1):
    return torch.softmax(x, dim=axis)
def _gelu(x):
    return torch.nn.functional.gelu(x, approximate="tanh")
def _silu(x):
    return torch.nn.functional.silu(x)
def _sigmoid(x):
    return torch.sigmoid(x)
def _layernorm(x, axis=-1, eps=1e-5):
    m = x.mean(dim=axis, keepdim=True)
    v = x.var(dim=axis, keepdim=True, unbiased=False)
    return (x - m) / torch.sqrt(v + eps)
def _scatter_add(tgt, idx, src, axis=0):
    return tgt.clone().index_add_(axis, idx, src)
def _segment_sum(src, seg, n):
    out = torch.zeros((n,) + src.shape[1:], dtype=src.dtype)
    return out.index_add_(0, seg, src)
def _pad(x, pw):
    flat = tuple(v for pair in reversed(pw) for v in pair)
    return torch.nn.functional.pad(x, flat)

kernel_name = 'standalone__normalfloat_lut_codebook_lookup__prompt'

def setup_inputs():

    _w = (lambda a: (lambda t: t.to(torch.bfloat16) if t.is_floating_point() else t)(torch.as_tensor(a)))
    B, S, D, F = 1, 2048, 1024, 3584
    G = 64 if 64 > 0 else D
    D = (D // G) * G
    ng = D // G
    nf4 = torch.tensor([
        -1.0, -0.6961928009986877, -0.5250730514526367, -0.39491748809814453,
        -0.28444138169288635, -0.18477343022823334, -0.09105003625154495, 0.0,
        0.07958029955625534, 0.16093020141124725, 0.24611230194568634, 0.33791524171829224,
        0.44070982933044434, 0.5626170039176941, 0.7229568362236023, 1.0
    ], dtype=float)
    qw = torch.randint(0, 16, (D, F)).to(torch.int64)
    absmax = (torch.abs((lambda *s: torch.randn(*s, dtype=torch.bfloat16) * 0.05)(ng, F)) + 0.01).to(float)
    return {
        "x": _w((lambda *s: torch.randn(*s, dtype=torch.bfloat16) * 0.05)(B, S, D)),
        "qw": _w(qw),
        "codebook": _w(nf4),
        "absmax": _w(absmax),
    }

def reference(x, qw, codebook, absmax):
    B, S, D = x.shape
    ng, F = absmax.shape
    G = D // ng
    w_nf = codebook[qw]
    s = (lambda x, n, axis: torch.repeat_interleave(x, n, dim=axis))(absmax, G, axis=0)
    w = (lambda x, dt: x.to(dt))(w_nf * s, x.dtype)
    return torch.einsum("bsd,df->bsf", x, w)
```

```cpp
#include <hip/hip_runtime.h>
#include <hip/hip_bf16.h>
#include <stdint.h>

typedef __attribute__((ext_vector_type(8))) short bf16x8;
typedef __attribute__((ext_vector_type(4))) float f32x4;
typedef __attribute__((ext_vector_type(4))) int i32x4;
typedef __attribute__((ext_vector_type(4))) unsigned u32x4;
typedef __attribute__((ext_vector_type(2))) unsigned u32x2;
typedef __attribute__((ext_vector_type(2))) float f32x2;
typedef __attribute__((ext_vector_type(2))) __bf16 bf16x2_t;
typedef __attribute__((address_space(1))) unsigned gu32;

constexpr int M_ = 2048, K_ = 1024, N_ = 3584;
constexpr int BM = 256, BN = 128, BK = 64, NT = K_ / BK;
constexpr int XB = BM * BK * 2;
constexpr int WB = BN * BK * 2;
constexpr int KCB = BN * 16;
constexpr int SB = XB + WB;
constexpr int NS = 3;
constexpr int HDR = 2048;
constexpr int LUT_OFF = -HDR;
constexpr int STG_OFF = SB + XB;
constexpr int SHM_BYTES = HDR + NS * SB;
constexpr size_t WIMG_BYTES = (size_t)28 * NT * WB;
constexpr size_t SYNC_OFF = (size_t)8 << 20;
constexpr unsigned SPIN_MAX = 1u << 17;
constexpr int POLLB_T = 5;
#define CTM 14208
constexpr unsigned CODE_TOUCH_MAX = CTM;
constexpr int AUX_NT = 2, AUX_SC1 = 16;

#define WAIT_V(n) asm volatile("s_waitcnt vmcnt(%0)" ::"n"(n) : "memory")
#define WAIT_L(n) asm volatile("s_waitcnt lgkmcnt(%0)" ::"n"(n) : "memory")
#define BAR() do { asm volatile("" ::: "memory"); __builtin_amdgcn_s_barrier(); asm volatile("" ::: "memory"); } while (0)
#define SCHED() __builtin_amdgcn_sched_barrier(0)
#define TAG_OF(e) ((e) * 0x9E3779B1u + 0x7F4A7C15u)
#define RLX_AGENT __ATOMIC_RELAXED, __HIP_MEMORY_SCOPE_AGENT
#define DAT(i)

__device__ __forceinline__ unsigned pack_bf16(float a, float b) {
  f32x2 v = {a, b};
  bf16x2_t r = __builtin_convertvector(v, bf16x2_t);
  return __builtin_bit_cast(unsigned, r);
}
__device__ __forceinline__ int kstep_of(int s, int tm, int tn) {
  const int u = (int)(0xFEDCBA9765432180ull >> (4 * s)) & 15;
  return (u & 8) + ((tm + u + tn) & 7);
}

struct QRegs { i32x4 r[8]; };
struct LV { f32x2 v[4][4]; float sc[4]; };
__device__ __forceinline__ f32x2 pair_lookup(const char* lut, int q0, int q1) { return *(const f32x2*)(lut + (q0 << 3) + (q1 << 7)); }
__device__ __forceinline__ u32x4 q_pack(const LV& l, int i) {
  u32x4 o;
  o.x = pack_bf16(l.v[i][0].x * l.sc[i], l.v[i][0].y * l.sc[i]); o.y = pack_bf16(l.v[i][1].x * l.sc[i], l.v[i][1].y * l.sc[i]);
  o.z = pack_bf16(l.v[i][2].x * l.sc[i], l.v[i][2].y * l.sc[i]); o.w = pack_bf16(l.v[i][3].x * l.sc[i], l.v[i][3].y * l.sc[i]);
  return o;
}

__global__ __launch_bounds__(512, 2) void nf4_gemm(const uint16_t* __restrict__ x, const int* __restrict__ qw,
                                                   const uint16_t* __restrict__ cb, const uint16_t* __restrict__ am,
                                                   char* __restrict__ ws, uint16_t* __restrict__ out) {
  __shared__ __attribute__((aligned(1024))) char shm_raw[SHM_BYTES];
  char* const shm = shm_raw + HDR;
  DAT(0);
  unsigned long long pc0; asm volatile("s_getpc_b64 %0" : "=s"(pc0));
  unsigned ct;
  asm volatile("global_load_dword %0, %1, %2" : "=&v"(ct) : "v"(min((unsigned)(threadIdx.x * 128u), CODE_TOUCH_MAX)), "s"(pc0) : "memory");
  const int bid = blockIdx.x;
  const int xcd = bid & 7, bi = bid >> 3;
  const int hi_ = bi >= 14, tn = (xcd >> 2) * 14 + bi - 14 * hi_, tm = (xcd & 3) * 2 + hi_;
  const int rot = (tm + tn) & 7;
  const int tid = threadIdx.x, lane = tid & 63;
  const int wid = __builtin_amdgcn_readfirstlane(tid >> 6);
  const int nc = lane & 31;
  const int kc0 = wid < 4 ? 2 * wid : 4 * (wid & 1);
  const unsigned qvoff = (unsigned)(((kc0 + (lane >> 5)) * 8 * N_ + nc * 4) * 4);
  const unsigned avoff = (unsigned)(nc * 8);
  const unsigned qs0 = (unsigned)(tn * BN * 4);
  { unsigned t0_ = qvoff, t1_ = avoff; asm volatile("" : "+v"(t0_), "+v"(t1_)); }
  asm volatile("" :: "s"(qs0 + (unsigned)rot * (BK * N_ * 4)));
  SCHED();
  asm volatile("" :: "s"(x), "s"(qw), "s"(cb), "s"(am), "s"(ws), "s"(out));
  gu32* flags = (gu32*)(ws + SYNC_OFF + (size_t)tn * 512);
  char* lut = shm + LUT_OFF;
  const __amdgpu_buffer_rsrc_t rsW = __builtin_amdgcn_make_buffer_rsrc((void*)ws, (short)0, (int)WIMG_BYTES, 0x00020000);
  const __amdgpu_buffer_rsrc_t rsQ = __builtin_amdgcn_make_buffer_rsrc((void*)qw, (short)0, K_ * N_ * 4, 0x00020000);
  const uint16_t* am0 = am + tn * BN;
  const unsigned zero = 0;
#define Q_ROW(Q, j, so_) asm volatile("buffer_load_dwordx4 %0, %1, %2, %3 offen nt" : "=&v"(Q.r[j]) : "v"(qvoff), "s"(rsQ), "s"((so_) + (j) * (N_ * 4)) : "memory")
#define Q_ROWS(Q, so_) do { _Pragma("unroll") for (int j_ = 0; j_ < 8; j_++) Q_ROW(Q, j_, so_); } while (0)
#define Q_SC(S, ks) asm volatile("global_load_dwordx2 %0, %1, %2" : "=&v"(S) : "v"(avoff), "s"(am0 + (ks) * N_) : "memory")
#define Q_SO(ks) (qs0 + (unsigned)(ks) * (BK * N_ * 4))
#define TAG_LOAD(T) asm volatile("global_load_dword %0, %1, %2 offset:256 sc1" : "=&v"(T) : "v"(zero), "s"(flags) : "memory")
#define Q_PIN(q) asm volatile("" : "+v"(q.r[0]), "+v"(q.r[1]), "+v"(q.r[2]), "+v"(q.r[3]), "+v"(q.r[4]), "+v"(q.r[5]), "+v"(q.r[6]), "+v"(q.r[7]))
#define SCALES(S) do { asm volatile("" : "+v"(S));                                                                                \
      lv.sc[0] = __builtin_bit_cast(float, S.x << 16); lv.sc[1] = __builtin_bit_cast(float, S.x & 0xffff0000u);                   \
      lv.sc[2] = __builtin_bit_cast(float, S.y << 16); lv.sc[3] = __builtin_bit_cast(float, S.y & 0xffff0000u); } while (0)
#define ROUND(Q, dst) do { Q_PIN(Q);                                                                                              \
      _Pragma("unroll") for (int i_ = 0; i_ < 4; i_++) _Pragma("unroll") for (int a_ = 0; a_ < 4; a_++)                           \
        lv.v[i_][a_] = pair_lookup(lut, Q.r[2 * a_][i_], Q.r[2 * a_ + 1][i_]);                                                    \
      SCHED();                                                                                                                    \
      char* d_ = shm + (dst) + io;                                                                             \
      _Pragma("unroll") for (int i_ = 0; i_ < 4; i_++) *(u32x4*)(d_ + ((i_ << 4) ^ isw)) = q_pack(lv, i_);                        \
      SCHED(); } while (0)
#define PUBLISH(src, ks, kcp) do { WAIT_L(0); {                                                                    \
        const unsigned g_ = (unsigned)((tn * NT + (ks)) * WB + (kcp) * (2 * KCB) + lane * 16); u32x4 pv_[4];                      \
        _Pragma("unroll") for (int i_ = 0; i_ < 4; i_++) pv_[i_] = *(const u32x4*)(shm + (src) + lane * 16 + i_ * 1024);           \
        _Pragma("unroll") for (int i_ = 0; i_ < 4; i_++) __builtin_amdgcn_raw_buffer_store_b128(pv_[i_], rsW, g_ + i_ * 1024, 0, AUX_SC1); } \
      SCHED(); } while (0)
  LV lv;

  if (__builtin_expect(wid < 4, 1)) {
    QRegs q; u32x2 qsv; unsigned c0, c1, tagv;
    asm volatile("global_load_ushort %0, %2, %4\n\tglobal_load_ushort %1, %3, %4" : "=&v"(c0), "=&v"(c1)
                 : "v"((unsigned)((lane & 15) * 2)), "v"((unsigned)((wid * 4 + (lane >> 4)) * 2)), "s"(cb) : "memory");
    Q_ROWS(q, Q_SO(rot));
    Q_SC(qsv, rot);
    TAG_LOAD(tagv);
    SCHED();
    BAR();
    const int p0 = 32 * (nc >> 3) + 16 * (nc & 1) + 4 * ((nc & 7) >> 1);
    const int io = (lane >> 5) * KCB + p0 * 16, isw = ((p0 >> 3) & 3) << 4;
    f32x4 acc[4][8];
#pragma unroll
    for (int m = 0; m < 4; m++)
#pragma unroll
      for (int n = 0; n < 8; n++) { acc[m][n] = (f32x4){0.f, 0.f, 0.f, 0.f}; asm volatile("" : "+v"(acc[m][n])); }
    WAIT_V(10);
    asm volatile("" : "+v"(c0), "+v"(c1)); asm volatile("" :: "v"(ct));
    *(u32x2*)(lut + tid * 8) = (u32x2){c0 << 16, c1 << 16};
    WAIT_L(0);
    BAR();
    WAIT_V(0);
    SCALES(qsv);
    ROUND(q, XB + kc0 * KCB);
    PUBLISH(XB + kc0 * KCB, rot, wid);
    BAR();
    const int fr = lane & 15, fq = lane >> 4;
    const int xo0 = (wid * 64 + fr) * 128 + ((fq ^ (fr >> 1)) << 4), xo1 = xo0 ^ 64;
    const int woe = XB + fq * KCB + ((fr ^ (fr >> 3)) << 4), woo = woe ^ 32;
    bf16x8 xa[4], wa[8], xb[4], wb[8];
#define RD(xf, wf, base, wbase_, xo, hf) do {                                                         \
      _Pragma("unroll") for (int n_ = 0; n_ < 8; n_++) wf[n_] = *(const bf16x8*)((wbase_) + ((n_ & 1) ? woo : woe) + (hf) * (4 * KCB) + n_ * 256); \
      _Pragma("unroll") for (int m_ = 0; m_ < 4; m_++) xf[m_] = *(const bf16x8*)((base) + (xo) + m_ * 2048); } while (0)
#define MM(xf, wf) do { _Pragma("unroll") for (int m_ = 0; m_ < 4; m_++) _Pragma("unroll") for (int n_ = 0; n_ < 8; n_++) \
      acc[m_][n_] = __builtin_amdgcn_mfma_f32_16x16x32_bf16(wf[n_], xf[m_], acc[m_][n_], 0, 0, 0); } while (0)
#define INTERLEAVE() do { _Pragma("unroll") for (int i_ = 0; i_ < 12; i_++) {                          \
      __builtin_amdgcn_sched_group_barrier(0x8, 2, 0); __builtin_amdgcn_sched_group_barrier(0x100, 1, 0); } \
      __builtin_amdgcn_sched_group_barrier(0x8, 8, 0); } while (0)
    WAIT_V(0);
    asm volatile("" : "+v"(tagv));
    if (lane == 0) __hip_atomic_store(flags + rot * 4 + wid, TAG_OF(tagv), RLX_AGENT);
    BAR();
#pragma unroll 1
    for (int ks = 0; ks < 2; ks++) {
      RD(xa, wa, shm, shm + ks * (4 * KCB), xo0 ^ (ks * 64), 0);
      MM(xa, wa);
      SCHED();
    }
    BAR();
    RD(xa, wa, shm + SB, shm + SB, xo0, 0);
    int buf = 1;
#pragma unroll 1
    for (int t = 1; t < NT - 3; t++) {
      const char* cur = shm + buf * SB;
      buf = buf == NS - 1 ? 0 : buf + 1;
      const char* nxt = shm + buf * SB;
      SCHED();
      RD(xb, wb, cur, cur, xo1, 1);
      MM(xa, wa);
      INTERLEAVE();
      SCHED();
      WAIT_L(0);
      BAR();
      SCHED();
      RD(xa, wa, nxt, nxt, xo0, 0);
      MM(xb, wb);
      INTERLEAVE();
      SCHED();
    }
#define RDH(xf, wf, base, xo, hf, mh) do {                                                            \
      _Pragma("unroll") for (int n_ = 0; n_ < 8; n_++) wf[n_] = *(const bf16x8*)((base) + ((n_ & 1) ? woo : woe) + (hf) * (4 * KCB) + n_ * 256); \
      _Pragma("unroll") for (int m_ = 0; m_ < 2; m_++) xf[m_] = *(const bf16x8*)((base) + (xo) + (2 * (mh) + m_) * 2048); } while (0)
#define MMH(xf, wf, mh) do { _Pragma("unroll") for (int m_ = 0; m_ < 2; m_++) _Pragma("unroll") for (int n_ = 0; n_ < 8; n_++) \
      acc[2 * (mh) + m_][n_] = __builtin_amdgcn_mfma_f32_16x16x32_bf16(wf[n_], xf[m_], acc[2 * (mh) + m_][n_], 0, 0, 0); } while (0)
#define INTERH() do { _Pragma("unroll") for (int i_ = 0; i_ < 10; i_++) {                               \
      __builtin_amdgcn_sched_group_barrier(0x8, 1, 0); __builtin_amdgcn_sched_group_barrier(0x100, 1, 0); } \
      __builtin_amdgcn_sched_group_barrier(0x8, 6, 0); SCHED(); } while (0)
#define EPI(mh) do { char* er = shm + SB + wid * 8192 + (mh) * 4096 + fr * 256;                       \
      _Pragma("unroll") for (int m_ = 0; m_ < 2; m_++) { const int m = 2 * (mh) + m_;                   \
        _Pragma("unroll") for (int P = 0; P < 4; P++) { u32x4 v;                                        \
          v.x = pack_bf16(acc[m][2 * P][0], acc[m][2 * P][1]); v.y = pack_bf16(acc[m][2 * P][2], acc[m][2 * P][3]);             \
          v.z = pack_bf16(acc[m][2 * P + 1][0], acc[m][2 * P + 1][1]); v.w = pack_bf16(acc[m][2 * P + 1][2], acc[m][2 * P + 1][3]); \
          *(u32x4*)(er + m_ * SB + (P >> 1) * 128 + ((P & 1) ? esw1 : esw0)) = v; } } } while (0)
    const int esw0 = (fq ^ (fr & 7)) << 4, esw1 = esw0 ^ 64;
    {
      const char* s13 = shm + SB; const char* s14 = shm + 2 * SB; const char* s15 = shm;
      SCHED();
      RDH(xb, wb, s13, xo1, 1, 0); MMH(xa, wa, 0); INTERH();
      BAR();
      SCHED();
      RDH(xa, wa, s14, xo0, 0, 0); MMH(xb, wb, 0); INTERH();
      RDH(xb, wb, s14, xo1, 1, 0); MMH(xa, wa, 0); INTERH();
      BAR();
      SCHED();
      RDH(xa, wa, s15, xo0, 0, 0); MMH(xb, wb, 0); INTERH();
      RDH(xb, wb, s15, xo1, 1, 0); MMH(xa, wa, 0); INTERH();
      RDH(xa, wa, s13, xo0, 0, 1); MMH(xb, wb, 0); INTERH();
      RDH(xb, wb, s13, xo1, 1, 1); MMH(xa, wa, 1); EPI(0);
#pragma unroll
      for (int i_ = 0; i_ < 16; i_++) {
        __builtin_amdgcn_sched_group_barrier(0x8, 1, 0);
        if (i_ < 10) __builtin_amdgcn_sched_group_barrier(0x100, 1, 0);
        if (i_ < 3) __builtin_amdgcn_sched_group_barrier(0x2, 4, 0); else __builtin_amdgcn_sched_group_barrier(0x2, 2, 0);
        if (i_ >= 8) __builtin_amdgcn_sched_group_barrier(0x200, 1, 0);
      }
      SCHED();
      WAIT_L(0);
      BAR();
      SCHED();
      RDH(xa, wa, s14, xo0, 0, 1); MMH(xb, wb, 1); INTERH();
      RDH(xb, wb, s14, xo1, 1, 1); MMH(xa, wa, 1); INTERH();
      RDH(xa, wa, s15, xo0, 0, 1); MMH(xb, wb, 1); INTERH();
      RDH(xb, wb, s15, xo1, 1, 1); MMH(xa, wa, 1); INTERH();
      MMH(xb, wb, 1);
      EPI(1);
    }
    WAIT_L(0);
  } else if (wid < 6) {
    const int h = wid - 4;
    unsigned xv[16];
#pragma unroll
    for (int i = 0; i < 16; i++) xv[i] = (unsigned)((8 * i + (lane >> 3)) * (K_ * 2) + (((lane & 7) ^ ((i & 1) * 4 + (lane >> 4))) << 4));
    const char* xbase = (const char*)(x + (size_t)(tm * BM + h * 128) * K_);
    const unsigned ldsx = (unsigned)(size_t)(shm) + h * 16384;
#define X8(o, l_, b_)                                                                                                             \
      asm volatile("s_mov_b32 m0, %8\n\ts_nop 0\n\tglobal_load_lds_dwordx4 %0, %9\n\t"                                            \
                   "s_add_u32 m0, m0, 0x400\n\ts_nop 0\n\tglobal_load_lds_dwordx4 %1, %9\n\t"                                     \
                   "s_add_u32 m0, m0, 0x400\n\ts_nop 0\n\tglobal_load_lds_dwordx4 %2, %9\n\t"                                     \
                   "s_add_u32 m0, m0, 0x400\n\ts_nop 0\n\tglobal_load_lds_dwordx4 %3, %9\n\t"                                     \
                   "s_add_u32 m0, m0, 0x400\n\ts_nop 0\n\tglobal_load_lds_dwordx4 %4, %9\n\t"                                     \
                   "s_add_u32 m0, m0, 0x400\n\ts_nop 0\n\tglobal_load_lds_dwordx4 %5, %9\n\t"                                     \
                   "s_add_u32 m0, m0, 0x400\n\ts_nop 0\n\tglobal_load_lds_dwordx4 %6, %9\n\t"                                     \
                   "s_add_u32 m0, m0, 0x400\n\ts_nop 0\n\tglobal_load_lds_dwordx4 %7, %9"                                         \
                   :: "v"(xv[o]), "v"(xv[o + 1]), "v"(xv[o + 2]), "v"(xv[o + 3]), "v"(xv[o + 4]), "v"(xv[o + 5]), "v"(xv[o + 6]), "v"(xv[o + 7]), \
                      "s"(l_), "s"(b_) : "memory", "scc")
#define ISSUE_X(s, slot) do { const char* b_ = xbase + kstep_of(s, tm, tn) * 128; const unsigned l_ = ldsx + (slot) * SB;       \
      X8(0, l_, b_); X8(8, l_ + 8192, b_); } while (0)
    BAR();
    BAR();
    BAR();
    ISSUE_X(0, 0);
    ISSUE_X(1, 1);
    WAIT_V(16);
    asm volatile("" :: "v"(ct));
    BAR();
    int xslot = 2;
#pragma unroll 1
    for (int t = -1; t < NT - 2; t++) {
      if (t + 3 < NT) { ISSUE_X(t + 3, xslot); xslot = xslot == NS - 1 ? 0 : xslot + 1; WAIT_V(16); } else { WAIT_V(0); }
      BAR();
    }
    BAR();
  } else {
    QRegs qa, qb; u32x2 sb; unsigned tagv;
    constexpr unsigned RO = 16 * N_ * 4;
    Q_SC(sb, rot + 8); TAG_LOAD(tagv);
    const int p0 = 32 * (nc >> 3) + 16 * (nc & 1) + 4 * ((nc & 7) >> 1);
    const int io = (lane >> 5) * KCB + p0 * 16, isw = ((p0 >> 3) & 3) << 4;
    BAR();
    Q_ROWS(qa, Q_SO(rot + 8));
    Q_ROWS(qb, Q_SO(rot + 8) + RO);
    BAR();
    BAR();
    WAIT_V(8);
    asm volatile("" :: "v"(ct));
    SCALES(sb);
    ROUND(qa, STG_OFF + kc0 * KCB);
    WAIT_V(0);
    ROUND(qb, STG_OFF + (kc0 + 2) * KCB);
    WAIT_L(0);
    asm volatile("" : "+v"(tagv));
    const unsigned tag = TAG_OF(tagv);
   if (wid == 6) {
    const char* wbase = ws + (size_t)tn * NT * WB;
    const unsigned wv = (unsigned)(lane * 16), wv2 = wv + 4096, wv3 = wv + 8192, wv4 = wv + 12288;
    const unsigned ldsw = (unsigned)(size_t)(shm) + XB;
#define ISSUE_W(s, slot) do { const char* b_ = wbase + (size_t)kstep_of(s, tm, tn) * WB; const unsigned l_ = ldsw + (slot) * SB; \
      asm volatile("s_mov_b32 m0, %4\n\ts_nop 0\n\t"                                                                              \
                   "global_load_lds_dwordx4 %0, %5 sc1\n\tglobal_load_lds_dwordx4 %0, %5 offset:1024 sc1\n\t"                     \
                   "global_load_lds_dwordx4 %0, %5 offset:2048 sc1\n\tglobal_load_lds_dwordx4 %0, %5 offset:3072 sc1\n\t"         \
                   "s_add_u32 m0, m0, 0x1000\n\ts_nop 0\n\t"                                                                      \
                   "global_load_lds_dwordx4 %1, %5 sc1\n\tglobal_load_lds_dwordx4 %1, %5 offset:1024 sc1\n\t"                     \
                   "global_load_lds_dwordx4 %1, %5 offset:2048 sc1\n\tglobal_load_lds_dwordx4 %1, %5 offset:3072 sc1\n\t"         \
                   "s_add_u32 m0, m0, 0x1000\n\ts_nop 0\n\t"                                                                      \
                   "global_load_lds_dwordx4 %2, %5 sc1\n\tglobal_load_lds_dwordx4 %2, %5 offset:1024 sc1\n\t"                     \
                   "global_load_lds_dwordx4 %2, %5 offset:2048 sc1\n\tglobal_load_lds_dwordx4 %2, %5 offset:3072 sc1\n\t"         \
                   "s_add_u32 m0, m0, 0x1000\n\ts_nop 0\n\t"                                                                      \
                   "global_load_lds_dwordx4 %3, %5 sc1\n\tglobal_load_lds_dwordx4 %3, %5 offset:1024 sc1\n\t"                     \
                   "global_load_lds_dwordx4 %3, %5 offset:2048 sc1\n\tglobal_load_lds_dwordx4 %3, %5 offset:3072 sc1"             \
                   :: "v"(wv), "v"(wv2), "v"(wv3), "v"(wv4), "s"(l_), "s"(b_) : "memory", "scc"); } while (0)
    const unsigned fvo = (unsigned)(lane * 4);
#define SET_RDY(f_) do { const unsigned long long b_ = __ballot((f_) == tag);                                                     \
      rda = (unsigned)__builtin_amdgcn_readfirstlane((unsigned)b_); rdb = (unsigned)__builtin_amdgcn_readfirstlane((unsigned)(b_ >> 32)); } while (0)
#define POLL_BLOCK() do { unsigned f_; asm volatile("global_load_dword %0, %1, %2 sc1\n\ts_waitcnt vmcnt(0)" : "=&v"(f_) : "v"(fvo), "s"(flags) : "memory"); \
      SET_RDY(f_); } while (0)
#define READY(s) (((((s) > 8 ? rdb : rda) >> ((kstep_of(s, tm, tn) & 7) * 4)) & 15u) == 15u)
#define NEXT_W() do { nw++; wslot = wslot == NS - 1 ? 0 : wslot + 1; } while (0)
    unsigned rda = 0, rdb = 0, spins = 0;
    BAR();
    int nw = 2, wslot = 2;
    unsigned fv = tagv;
    bool ppoll = false, preg = false;
#pragma unroll 1
    for (int t = -1; t < NT - 2; t++) {
      const int lim = t + 3 < NT ? t + 3 : NT - 1;
      if (preg) { WAIT_V(16); } else { WAIT_V(0); }
      asm volatile("" : "+v"(fv));
      if (ppoll) SET_RDY(fv);
      if (nw <= t + 2) {
#pragma unroll 1
        while (!READY(nw)) { if (++spins > SPIN_MAX) { rda = ~0u; rdb = ~0u; break; } POLL_BLOCK(); }
        ISSUE_W(nw, wslot); NEXT_W();
      }
      ppoll = spins <= SPIN_MAX && (t < POLLB_T ? rda != ~0u : rdb != ~0u);
      asm volatile("global_load_dword %0, %1, %2 sc1" : "+v"(fv) : "v"(fvo), "s"(ppoll ? (const char*)flags : (const char*)am) : "memory");
      preg = nw <= lim && READY(nw);
      if (preg) { ISSUE_W(nw, wslot); NEXT_W(); WAIT_V(17); } else { WAIT_V(1); }
      BAR();
    }
    WAIT_V(0);
    asm volatile("" : "+v"(fv));
    if (tm == 0) {
#pragma unroll 1
      while ((rda & rdb) != ~0u && ++spins < SPIN_MAX) POLL_BLOCK();
      if (lane == 0) __hip_atomic_store(flags + 64, tag, RLX_AGENT);
    }
    BAR();
   } else {
    BAR();
    u32x4 bv[16];
#pragma unroll
    for (int i = 0; i < 16; i++) bv[i] = *(const u32x4*)(shm + STG_OFF + lane * 16 + i * 1024);
    WAIT_L(0);
    SCHED();
    BAR();
    const unsigned g_ = (unsigned)((tn * NT + rot + 8) * WB + lane * 16);
#pragma unroll
    for (int i = 0; i < 8; i++) __builtin_amdgcn_raw_buffer_store_b128(bv[i], rsW, g_ + i * 1024, 0, AUX_SC1);
    SCHED();
    BAR();
#pragma unroll
    for (int i = 8; i < 16; i++) __builtin_amdgcn_raw_buffer_store_b128(bv[i], rsW, g_ + i * 1024, 0, AUX_SC1);
    SCHED();
    BAR();
    BAR();
    WAIT_V(0);
    if (lane < 4) __hip_atomic_store(flags + (rot + 8) * 4 + lane, tag, RLX_AGENT);
#pragma unroll 1
    for (int t = 4; t < NT; t++) BAR();
   }
  }
  {
    const __amdgpu_buffer_rsrc_t rsO = __builtin_amdgcn_make_buffer_rsrc((void*)out, (short)0, M_ * N_ * 2, 0x00020000);
    const unsigned so = (unsigned)(((tm * BM + (wid & 3) * 64 + (wid < 4 ? 32 : 0)) * N_ + tn * BN) * 2);
    const char* sb = shm + SB + (wid & 3) * 8192 + (wid < 4 ? 4096 : 0);
    const int r4 = lane >> 4, c = lane & 15;
    DAT(24);
    u32x4 ov[8];
#pragma unroll
    for (int i = 0; i < 8; i++) ov[i] = *(const u32x4*)(sb + (i >> 2) * SB + (r4 + (i & 3) * 4) * 256 + ((c ^ (r4 + (i & 1) * 4)) << 4));
    unsigned vf = so + (unsigned)(r4 * (N_ * 2) + c * 16);
    asm volatile("" : "+v"(vf));
#pragma unroll
    for (int i = 0; i < 8; i++) __builtin_amdgcn_raw_buffer_store_b128(ov[i], rsO, vf + i * 4 * (N_ * 2), 0, AUX_NT);
  }
}

extern "C" void kernel_launch(void* const* d_in, const int* in_sizes, int n_in, void* d_out, int out_size, void* d_ws,
                              size_t ws_size, hipStream_t stream) {
  nf4_gemm<<<224, 512, 0, stream>>>((const uint16_t*)d_in[0], (const int*)d_in[1], (const uint16_t*)d_in[2],
                                    (const uint16_t*)d_in[3], (char*)d_ws, (uint16_t*)d_out);
}
```

```cpp
#include <hip/hip_runtime.h>
#include <hip/hip_bf16.h>
#include <stdint.h>

typedef __attribute__((ext_vector_type(8))) short bf16x8;
typedef __attribute__((ext_vector_type(4))) float f32x4;
typedef __attribute__((ext_vector_type(4))) int i32x4;
typedef __attribute__((ext_vector_type(4))) unsigned u32x4;
typedef __attribute__((ext_vector_type(2))) unsigned u32x2;
typedef __attribute__((ext_vector_type(2))) float f32x2;
typedef __attribute__((ext_vector_type(2))) __bf16 bf16x2_t;
typedef __attribute__((address_space(1))) unsigned gu32;

constexpr int M_ = 2048, K_ = 1024, N_ = 3584;
constexpr int BM = 256, BN = 128, BK = 64, NT = K_ / BK;
constexpr int XB = BM * BK * 2;
constexpr int WB = BN * BK * 2;
constexpr int KCB = BN * 16;
constexpr int SB = XB + WB;
constexpr int NS = 3;
constexpr int HDR = 2048;
constexpr int LUT_OFF = -HDR;
constexpr int STG_OFF = SB + XB;
constexpr int SHM_BYTES = HDR + NS * SB;
constexpr size_t WIMG_BYTES = (size_t)28 * NT * WB;
constexpr size_t SYNC_OFF = (size_t)8 << 20;
constexpr unsigned SPIN_MAX = 1u << 17;
constexpr int POLLB_T = 5;
constexpr unsigned CODE_TOUCH_MAX = 11904;
constexpr int AUX_NT = 2, AUX_SC1 = 16;

#define WAIT_V(n) asm volatile("s_waitcnt vmcnt(%0)" ::"n"(n) : "memory")
#define WAIT_L(n) asm volatile("s_waitcnt lgkmcnt(%0)" ::"n"(n) : "memory")
#define BAR() do { asm volatile("" ::: "memory"); __builtin_amdgcn_s_barrier(); asm volatile("" ::: "memory"); } while (0)
#define SCHED() __builtin_amdgcn_sched_barrier(0)
#define TAG_OF(e) ((e) * 0x9E3779B1u + 0x7F4A7C15u)
#define RLX_AGENT __ATOMIC_RELAXED, __HIP_MEMORY_SCOPE_AGENT

__device__ __forceinline__ unsigned pack_bf16(float a, float b) {
  f32x2 v = {a, b};
  bf16x2_t r = __builtin_convertvector(v, bf16x2_t);
  return __builtin_bit_cast(unsigned, r);
}
__device__ __forceinline__ int kstep_of(int s, int tm, int tn) {
  const int u = (int)(0xFEDCBA9765432180ull >> (4 * s)) & 15;
  return (u & 8) + ((tm + u + tn) & 7);
}

struct QRegs { i32x4 r[8]; };
struct LV { f32x2 v[4][4]; float sc[4]; };
__device__ __forceinline__ f32x2 pair_lookup(const char* lut, int q0, int q1) { return *(const f32x2*)(lut + (q0 << 3) + (q1 << 7)); }
__device__ __forceinline__ u32x4 q_pack(const LV& l, int i) {
  u32x4 o;
  o.x = pack_bf16(l.v[i][0].x * l.sc[i], l.v[i][0].y * l.sc[i]); o.y = pack_bf16(l.v[i][1].x * l.sc[i], l.v[i][1].y * l.sc[i]);
  o.z = pack_bf16(l.v[i][2].x * l.sc[i], l.v[i][2].y * l.sc[i]); o.w = pack_bf16(l.v[i][3].x * l.sc[i], l.v[i][3].y * l.sc[i]);
  return o;
}

__global__ __launch_bounds__(512, 2) void nf4_gemm(const uint16_t* __restrict__ x, const int* __restrict__ qw,
                                                   const uint16_t* __restrict__ cb, const uint16_t* __restrict__ am,
                                                   char* __restrict__ ws, uint16_t* __restrict__ out) {
  __shared__ __attribute__((aligned(1024))) char shm_raw[SHM_BYTES];
  char* const shm = shm_raw + HDR;
  unsigned long long pc0; asm volatile("s_getpc_b64 %0" : "=s"(pc0));
  unsigned ct;
  asm volatile("global_load_dword %0, %1, %2" : "=&v"(ct) : "v"(min((unsigned)(threadIdx.x * 128u), CODE_TOUCH_MAX)), "s"(pc0) : "memory");
  const int bid = blockIdx.x;
  const int xcd = bid & 7, bi = bid >> 3;
  const int hi_ = bi >= 14, tn = (xcd >> 2) * 14 + bi - 14 * hi_, tm = (xcd & 3) * 2 + hi_;
  const int rot = (tm + tn) & 7;
  const int tid = threadIdx.x, lane = tid & 63;
  const int wid = __builtin_amdgcn_readfirstlane(tid >> 6);
  const int nc = lane & 31;
  const int kc0 = wid < 4 ? 2 * wid : 4 * (wid & 1);
  const unsigned qvoff = (unsigned)(((kc0 + (lane >> 5)) * 8 * N_ + nc * 4) * 4);
  const unsigned avoff = (unsigned)(nc * 8);
  const unsigned qs0 = (unsigned)(tn * BN * 4);
  { unsigned t0_ = qvoff, t1_ = avoff; asm volatile("" : "+v"(t0_), "+v"(t1_)); }
  asm volatile("" :: "s"(qs0 + (unsigned)rot * (BK * N_ * 4)));
  SCHED();
  asm volatile("" :: "s"(x), "s"(qw), "s"(cb), "s"(am), "s"(ws), "s"(out));
  gu32* flags = (gu32*)(ws + SYNC_OFF + (size_t)tn * 512);
  char* lut = shm + LUT_OFF;
  const __amdgpu_buffer_rsrc_t rsW = __builtin_amdgcn_make_buffer_rsrc((void*)ws, (short)0, (int)WIMG_BYTES, 0x00020000);
  const __amdgpu_buffer_rsrc_t rsQ = __builtin_amdgcn_make_buffer_rsrc((void*)qw, (short)0, K_ * N_ * 4, 0x00020000);
  const uint16_t* am0 = am + tn * BN;
  const unsigned zero = 0;
#define Q_ROW(Q, j, so_) asm volatile("buffer_load_dwordx4 %0, %1, %2, %3 offen nt" : "=&v"(Q.r[j]) : "v"(qvoff), "s"(rsQ), "s"((so_) + (j) * (N_ * 4)) : "memory")
#define Q_ROWS(Q, so_) do { _Pragma("unroll") for (int j_ = 0; j_ < 8; j_++) Q_ROW(Q, j_, so_); } while (0)
#define Q_SC(S, ks) asm volatile("global_load_dwordx2 %0, %1, %2" : "=&v"(S) : "v"(avoff), "s"(am0 + (ks) * N_) : "memory")
#define Q_SO(ks) (qs0 + (unsigned)(ks) * (BK * N_ * 4))
#define TAG_LOAD(T) asm volatile("global_load_dword %0, %1, %2 offset:256 sc1" : "=&v"(T) : "v"(zero), "s"(flags) : "memory")
#define Q_PIN(q) asm volatile("" : "+v"(q.r[0]), "+v"(q.r[1]), "+v"(q.r[2]), "+v"(q.r[3]), "+v"(q.r[4]), "+v"(q.r[5]), "+v"(q.r[6]), "+v"(q.r[7]))
#define SCALES(S) do { asm volatile("" : "+v"(S));                                                                                \
      lv.sc[0] = __builtin_bit_cast(float, S.x << 16); lv.sc[1] = __builtin_bit_cast(float, S.x & 0xffff0000u);                   \
      lv.sc[2] = __builtin_bit_cast(float, S.y << 16); lv.sc[3] = __builtin_bit_cast(float, S.y & 0xffff0000u); } while (0)
#define ROUND(Q, dst) do { Q_PIN(Q);                                                                                              \
      _Pragma("unroll") for (int i_ = 0; i_ < 4; i_++) _Pragma("unroll") for (int a_ = 0; a_ < 4; a_++)                           \
        lv.v[i_][a_] = pair_lookup(lut, Q.r[2 * a_][i_], Q.r[2 * a_ + 1][i_]);                                                    \
      SCHED();                                                                                                                    \
      char* d_ = shm + (dst) + io;                                                                             \
      _Pragma("unroll") for (int i_ = 0; i_ < 4; i_++) *(u32x4*)(d_ + ((i_ << 4) ^ isw)) = q_pack(lv, i_);                        \
      SCHED(); } while (0)
#define PUBLISH(src, ks, kcp) do { WAIT_L(0); {                                                                    \
        const unsigned g_ = (unsigned)((tn * NT + (ks)) * WB + (kcp) * (2 * KCB) + lane * 16); u32x4 pv_[4];                      \
        _Pragma("unroll") for (int i_ = 0; i_ < 4; i_++) pv_[i_] = *(const u32x4*)(shm + (src) + lane * 16 + i_ * 1024);           \
        _Pragma("unroll") for (int i_ = 0; i_ < 4; i_++) __builtin_amdgcn_raw_buffer_store_b128(pv_[i_], rsW, g_ + i_ * 1024, 0, AUX_SC1); } \
      SCHED(); } while (0)
  LV lv;

  if (__builtin_expect(wid < 4, 1)) {
    QRegs q; u32x2 qsv; unsigned c0, c1, tagv;
    asm volatile("global_load_ushort %0, %2, %4\n\tglobal_load_ushort %1, %3, %4" : "=&v"(c0), "=&v"(c1)
                 : "v"((unsigned)((lane & 15) * 2)), "v"((unsigned)((wid * 4 + (lane >> 4)) * 2)), "s"(cb) : "memory");
    Q_ROWS(q, Q_SO(rot));
    Q_SC(qsv, rot);
    TAG_LOAD(tagv);
    SCHED();
    BAR();
    const int p0 = 32 * (nc >> 3) + 16 * (nc & 1) + 4 * ((nc & 7) >> 1);
    const int io = (lane >> 5) * KCB + p0 * 16, isw = ((p0 >> 3) & 3) << 4;
    f32x4 acc[4][8];
#pragma unroll
    for (int m = 0; m < 4; m++)
#pragma unroll
      for (int n = 0; n < 8; n++) { acc[m][n] = (f32x4){0.f, 0.f, 0.f, 0.f}; asm volatile("" : "+v"(acc[m][n])); }
    WAIT_V(10);
    asm volatile("" : "+v"(c0), "+v"(c1)); asm volatile("" :: "v"(ct));
    *(u32x2*)(lut + tid * 8) = (u32x2){c0 << 16, c1 << 16};
    WAIT_L(0);
    BAR();
    WAIT_V(0);
    SCALES(qsv);
    ROUND(q, XB + kc0 * KCB);
    PUBLISH(XB + kc0 * KCB, rot, wid);
    BAR();
    const int fr = lane & 15, fq = lane >> 4;
    const int xo0 = (wid * 64 + fr) * 128 + ((fq ^ (fr >> 1)) << 4), xo1 = xo0 ^ 64;
    const int woe = XB + fq * KCB + ((fr ^ (fr >> 3)) << 4), woo = woe ^ 32;
    bf16x8 xa[4], wa[8], xb[4], wb[8];
#define RD(xf, wf, base, wbase_, xo, hf) do {                                                         \
      _Pragma("unroll") for (int n_ = 0; n_ < 8; n_++) wf[n_] = *(const bf16x8*)((wbase_) + ((n_ & 1) ? woo : woe) + (hf) * (4 * KCB) + n_ * 256); \
      _Pragma("unroll") for (int m_ = 0; m_ < 4; m_++) xf[m_] = *(const bf16x8*)((base) + (xo) + m_ * 2048); } while (0)
#define MM(xf, wf) do { _Pragma("unroll") for (int m_ = 0; m_ < 4; m_++) _Pragma("unroll") for (int n_ = 0; n_ < 8; n_++) \
      acc[m_][n_] = __builtin_amdgcn_mfma_f32_16x16x32_bf16(wf[n_], xf[m_], acc[m_][n_], 0, 0, 0); } while (0)
#define INTERLEAVE() do { _Pragma("unroll") for (int i_ = 0; i_ < 12; i_++) {                          \
      __builtin_amdgcn_sched_group_barrier(0x8, 2, 0); __builtin_amdgcn_sched_group_barrier(0x100, 1, 0); } \
      __builtin_amdgcn_sched_group_barrier(0x8, 8, 0); } while (0)
    WAIT_V(0);
    asm volatile("" : "+v"(tagv));
    if (lane == 0) __hip_atomic_store(flags + rot * 4 + wid, TAG_OF(tagv), RLX_AGENT);
    BAR();
#pragma unroll 1
    for (int ks = 0; ks < 2; ks++) {
      RD(xa, wa, shm, shm + ks * (4 * KCB), xo0 ^ (ks * 64), 0);
      MM(xa, wa);
      SCHED();
    }
    BAR();
    RD(xa, wa, shm + SB, shm + SB, xo0, 0);
    int buf = 1;
#pragma unroll 1
    for (int t = 1; t < NT - 3; t++) {
      const char* cur = shm + buf * SB;
      buf = buf == NS - 1 ? 0 : buf + 1;
      const char* nxt = shm + buf * SB;
      SCHED();
      RD(xb, wb, cur, cur, xo1, 1);
      MM(xa, wa);
      INTERLEAVE();
      SCHED();
      WAIT_L(0);
      BAR();
      SCHED();
      RD(xa, wa, nxt, nxt, xo0, 0);
      MM(xb, wb);
      INTERLEAVE();
      SCHED();
    }
#define RDH(xf, wf, base, xo, hf, mh) do {                                                            \
      _Pragma("unroll") for (int n_ = 0; n_ < 8; n_++) wf[n_] = *(const bf16x8*)((base) + ((n_ & 1) ? woo : woe) + (hf) * (4 * KCB) + n_ * 256); \
      _Pragma("unroll") for (int m_ = 0; m_ < 2; m_++) xf[m_] = *(const bf16x8*)((base) + (xo) + (2 * (mh) + m_) * 2048); } while (0)
#define MMH(xf, wf, mh) do { _Pragma("unroll") for (int m_ = 0; m_ < 2; m_++) _Pragma("unroll") for (int n_ = 0; n_ < 8; n_++) \
      acc[2 * (mh) + m_][n_] = __builtin_amdgcn_mfma_f32_16x16x32_bf16(wf[n_], xf[m_], acc[2 * (mh) + m_][n_], 0, 0, 0); } while (0)
#define INTERH() do { _Pragma("unroll") for (int i_ = 0; i_ < 10; i_++) {                               \
      __builtin_amdgcn_sched_group_barrier(0x8, 1, 0); __builtin_amdgcn_sched_group_barrier(0x100, 1, 0); } \
      __builtin_amdgcn_sched_group_barrier(0x8, 6, 0); SCHED(); } while (0)
#define EPI(mh) do { char* er = shm + SB + wid * 8192 + (mh) * 4096 + fr * 256;                       \
      _Pragma("unroll") for (int m_ = 0; m_ < 2; m_++) { const int m = 2 * (mh) + m_;                   \
        _Pragma("unroll") for (int P = 0; P < 4; P++) { u32x4 v;                                        \
          v.x = pack_bf16(acc[m][2 * P][0], acc[m][2 * P][1]); v.y = pack_bf16(acc[m][2 * P][2], acc[m][2 * P][3]);             \
          v.z = pack_bf16(acc[m][2 * P + 1][0], acc[m][2 * P + 1][1]); v.w = pack_bf16(acc[m][2 * P + 1][2], acc[m][2 * P + 1][3]); \
          *(u32x4*)(er + m_ * SB + (P >> 1) * 128 + ((P & 1) ? esw1 : esw0)) = v; } } } while (0)
    const int esw0 = (fq ^ (fr & 7)) << 4, esw1 = esw0 ^ 64;
    {
      const char* s13 = shm + SB; const char* s14 = shm + 2 * SB; const char* s15 = shm;
      SCHED();
      RDH(xb, wb, s13, xo1, 1, 0); MMH(xa, wa, 0); INTERH();
      BAR();
      SCHED();
      RDH(xa, wa, s14, xo0, 0, 0); MMH(xb, wb, 0); INTERH();
      RDH(xb, wb, s14, xo1, 1, 0); MMH(xa, wa, 0); INTERH();
      BAR();
      SCHED();
      RDH(xa, wa, s15, xo0, 0, 0); MMH(xb, wb, 0); INTERH();
      RDH(xb, wb, s15, xo1, 1, 0); MMH(xa, wa, 0); INTERH();
      RDH(xa, wa, s13, xo0, 0, 1); MMH(xb, wb, 0); INTERH();
      RDH(xb, wb, s13, xo1, 1, 1); MMH(xa, wa, 1); EPI(0);
#pragma unroll
      for (int i_ = 0; i_ < 16; i_++) {
        __builtin_amdgcn_sched_group_barrier(0x8, 1, 0);
        if (i_ < 10) __builtin_amdgcn_sched_group_barrier(0x100, 1, 0);
        if (i_ < 3) __builtin_amdgcn_sched_group_barrier(0x2, 4, 0); else __builtin_amdgcn_sched_group_barrier(0x2, 2, 0);
        if (i_ >= 8) __builtin_amdgcn_sched_group_barrier(0x200, 1, 0);
      }
      SCHED();
      WAIT_L(0);
      BAR();
      SCHED();
      RDH(xa, wa, s14, xo0, 0, 1); MMH(xb, wb, 1); INTERH();
      RDH(xb, wb, s14, xo1, 1, 1); MMH(xa, wa, 1); INTERH();
      RDH(xa, wa, s15, xo0, 0, 1); MMH(xb, wb, 1); INTERH();
      RDH(xb, wb, s15, xo1, 1, 1); MMH(xa, wa, 1); INTERH();
      MMH(xb, wb, 1);
      EPI(1);
    }
    WAIT_L(0);
  } else if (wid < 6) {
    const int h = wid - 4;
    unsigned xv[16];
#pragma unroll
    for (int i = 0; i < 16; i++) xv[i] = (unsigned)((8 * i + (lane >> 3)) * (K_ * 2) + (((lane & 7) ^ ((i & 1) * 4 + (lane >> 4))) << 4));
    const char* xbase = (const char*)(x + (size_t)(tm * BM + h * 128) * K_);
    const unsigned ldsx = (unsigned)(size_t)(shm) + h * 16384;
#define X8(o, l_, b_)                                                                                                             \
      asm volatile("s_mov_b32 m0, %8\n\ts_nop 0\n\tglobal_load_lds_dwordx4 %0, %9\n\t"                                            \
                   "s_add_u32 m0, m0, 0x400\n\ts_nop 0\n\tglobal_load_lds_dwordx4 %1, %9\n\t"                                     \
                   "s_add_u32 m0, m0, 0x400\n\ts_nop 0\n\tglobal_load_lds_dwordx4 %2, %9\n\t"                                     \
                   "s_add_u32 m0, m0, 0x400\n\ts_nop 0\n\tglobal_load_lds_dwordx4 %3, %9\n\t"                                     \
                   "s_add_u32 m0, m0, 0x400\n\ts_nop 0\n\tglobal_load_lds_dwordx4 %4, %9\n\t"                                     \
                   "s_add_u32 m0, m0, 0x400\n\ts_nop 0\n\tglobal_load_lds_dwordx4 %5, %9\n\t"                                     \
                   "s_add_u32 m0, m0, 0x400\n\ts_nop 0\n\tglobal_load_lds_dwordx4 %6, %9\n\t"                                     \
                   "s_add_u32 m0, m0, 0x400\n\ts_nop 0\n\tglobal_load_lds_dwordx4 %7, %9"                                         \
                   :: "v"(xv[o]), "v"(xv[o + 1]), "v"(xv[o + 2]), "v"(xv[o + 3]), "v"(xv[o + 4]), "v"(xv[o + 5]), "v"(xv[o + 6]), "v"(xv[o + 7]), \
                      "s"(l_), "s"(b_) : "memory", "scc")
#define ISSUE_X(s, slot) do { const char* b_ = xbase + kstep_of(s, tm, tn) * 128; const unsigned l_ = ldsx + (slot) * SB;       \
      X8(0, l_, b_); X8(8, l_ + 8192, b_); } while (0)
    BAR();
    BAR();
    BAR();
    ISSUE_X(0, 0);
    ISSUE_X(1, 1);
    WAIT_V(16);
    asm volatile("" :: "v"(ct));
    BAR();
    int xslot = 2;
#pragma unroll 1
    for (int t = -1; t < NT - 2; t++) {
      if (t + 3 < NT) { ISSUE_X(t + 3, xslot); xslot = xslot == NS - 1 ? 0 : xslot + 1; WAIT_V(16); } else { WAIT_V(0); }
      BAR();
    }
    BAR();
  } else {
    QRegs qa, qb; u32x2 sb; unsigned tagv;
    constexpr unsigned RO = 16 * N_ * 4;
    Q_SC(sb, rot + 8); TAG_LOAD(tagv);
    const int p0 = 32 * (nc >> 3) + 16 * (nc & 1) + 4 * ((nc & 7) >> 1);
    const int io = (lane >> 5) * KCB + p0 * 16, isw = ((p0 >> 3) & 3) << 4;
    BAR();
    Q_ROWS(qa, Q_SO(rot + 8));
    Q_ROWS(qb, Q_SO(rot + 8) + RO);
    BAR();
    BAR();
    WAIT_V(8);
    asm volatile("" :: "v"(ct));
    SCALES(sb);
    ROUND(qa, STG_OFF + kc0 * KCB);
    WAIT_V(0);
    ROUND(qb, STG_OFF + (kc0 + 2) * KCB);
    WAIT_L(0);
    asm volatile("" : "+v"(tagv));
    const unsigned tag = TAG_OF(tagv);
   if (wid == 6) {
    const char* wbase = ws + (size_t)tn * NT * WB;
    const unsigned wv = (unsigned)(lane * 16), wv2 = wv + 4096, wv3 = wv + 8192, wv4 = wv + 12288;
    const unsigned ldsw = (unsigned)(size_t)(shm) + XB;
#define ISSUE_W(s, slot) do { const char* b_ = wbase + (size_t)kstep_of(s, tm, tn) * WB; const unsigned l_ = ldsw + (slot) * SB; \
      asm volatile("s_mov_b32 m0, %4\n\ts_nop 0\n\t"                                                                              \
                   "global_load_lds_dwordx4 %0, %5 sc1\n\tglobal_load_lds_dwordx4 %0, %5 offset:1024 sc1\n\t"                     \
                   "global_load_lds_dwordx4 %0, %5 offset:2048 sc1\n\tglobal_load_lds_dwordx4 %0, %5 offset:3072 sc1\n\t"         \
                   "s_add_u32 m0, m0, 0x1000\n\ts_nop 0\n\t"                                                                      \
                   "global_load_lds_dwordx4 %1, %5 sc1\n\tglobal_load_lds_dwordx4 %1, %5 offset:1024 sc1\n\t"                     \
                   "global_load_lds_dwordx4 %1, %5 offset:2048 sc1\n\tglobal_load_lds_dwordx4 %1, %5 offset:3072 sc1\n\t"         \
                   "s_add_u32 m0, m0, 0x1000\n\ts_nop 0\n\t"                                                                      \
                   "global_load_lds_dwordx4 %2, %5 sc1\n\tglobal_load_lds_dwordx4 %2, %5 offset:1024 sc1\n\t"                     \
                   "global_load_lds_dwordx4 %2, %5 offset:2048 sc1\n\tglobal_load_lds_dwordx4 %2, %5 offset:3072 sc1\n\t"         \
                   "s_add_u32 m0, m0, 0x1000\n\ts_nop 0\n\t"                                                                      \
                   "global_load_lds_dwordx4 %3, %5 sc1\n\tglobal_load_lds_dwordx4 %3, %5 offset:1024 sc1\n\t"                     \
                   "global_load_lds_dwordx4 %3, %5 offset:2048 sc1\n\tglobal_load_lds_dwordx4 %3, %5 offset:3072 sc1"             \
                   :: "v"(wv), "v"(wv2), "v"(wv3), "v"(wv4), "s"(l_), "s"(b_) : "memory", "scc"); } while (0)
    const unsigned fvo = (unsigned)(lane * 4);
#define SET_RDY(f_) do { const unsigned long long b_ = __ballot((f_) == tag);                                                     \
      rda = (unsigned)__builtin_amdgcn_readfirstlane((unsigned)b_); rdb = (unsigned)__builtin_amdgcn_readfirstlane((unsigned)(b_ >> 32)); } while (0)
#define POLL_BLOCK() do { unsigned f_; asm volatile("global_load_dword %0, %1, %2 sc1\n\ts_waitcnt vmcnt(0)" : "=&v"(f_) : "v"(fvo), "s"(flags) : "memory"); \
      SET_RDY(f_); } while (0)
#define READY(s) (((((s) > 8 ? rdb : rda) >> ((kstep_of(s, tm, tn) & 7) * 4)) & 15u) == 15u)
#define NEXT_W() do { nw++; wslot = wslot == NS - 1 ? 0 : wslot + 1; } while (0)
    unsigned rda = 0, rdb = 0, spins = 0;
    BAR();
    int nw = 2, wslot = 2;
    unsigned fv = tagv;
    bool ppoll = false, preg = false;
#pragma unroll 1
    for (int t = -1; t < NT - 2; t++) {
      const int lim = t + 3 < NT ? t + 3 : NT - 1;
      if (preg) { WAIT_V(16); } else { WAIT_V(0); }
      asm volatile("" : "+v"(fv));
      if (ppoll) SET_RDY(fv);
      if (nw <= t + 2) {
#pragma unroll 1
        while (!READY(nw)) { if (++spins > SPIN_MAX) { rda = ~0u; rdb = ~0u; break; } POLL_BLOCK(); }
        ISSUE_W(nw, wslot); NEXT_W();
      }
      ppoll = spins <= SPIN_MAX && (t < POLLB_T ? rda != ~0u : rdb != ~0u);
      asm volatile("global_load_dword %0, %1, %2 sc1" : "+v"(fv) : "v"(fvo), "s"(ppoll ? (const char*)flags : (const char*)am) : "memory");
      preg = nw <= lim && READY(nw);
      if (preg) { ISSUE_W(nw, wslot); NEXT_W(); WAIT_V(17); } else { WAIT_V(1); }
      BAR();
    }
    WAIT_V(0);
    asm volatile("" : "+v"(fv));
    if (tm == 0) {
#pragma unroll 1
      while ((rda & rdb) != ~0u && ++spins < SPIN_MAX) POLL_BLOCK();
      if (lane == 0) __hip_atomic_store(flags + 64, tag, RLX_AGENT);
    }
    BAR();
   } else {
    BAR();
    u32x4 bv[16];
#pragma unroll
    for (int i = 0; i < 16; i++) bv[i] = *(const u32x4*)(shm + STG_OFF + lane * 16 + i * 1024);
    WAIT_L(0);
    SCHED();
    BAR();
    const unsigned g_ = (unsigned)((tn * NT + rot + 8) * WB + lane * 16);
#pragma unroll
    for (int i = 0; i < 8; i++) __builtin_amdgcn_raw_buffer_store_b128(bv[i], rsW, g_ + i * 1024, 0, AUX_SC1);
    SCHED();
    BAR();
#pragma unroll
    for (int i = 8; i < 16; i++) __builtin_amdgcn_raw_buffer_store_b128(bv[i], rsW, g_ + i * 1024, 0, AUX_SC1);
    SCHED();
    BAR();
    BAR();
    WAIT_V(0);
    if (lane < 4) __hip_atomic_store(flags + (rot + 8) * 4 + lane, tag, RLX_AGENT);
#pragma unroll 1
    for (int t = 4; t < NT; t++) BAR();
   }
  }
  {
    const __amdgpu_buffer_rsrc_t rsO = __builtin_amdgcn_make_buffer_rsrc((void*)out, (short)0, M_ * N_ * 2, 0x00020000);
    const unsigned so = (unsigned)(((tm * BM + (wid & 3) * 64 + (wid < 4 ? 32 : 0)) * N_ + tn * BN) * 2);
    const char* sb = shm + SB + (wid & 3) * 8192 + (wid < 4 ? 4096 : 0);
    const int r4 = lane >> 4, c = lane & 15;
    u32x4 ov[8];
#pragma unroll
    for (int i = 0; i < 8; i++) ov[i] = *(const u32x4*)(sb + (i >> 2) * SB + (r4 + (i & 3) * 4) * 256 + ((c ^ (r4 + (i & 1) * 4)) << 4));
    unsigned vf = so + (unsigned)(r4 * (N_ * 2) + c * 16);
    asm volatile("" : "+v"(vf));
#pragma unroll
    for (int i = 0; i < 8; i++) __builtin_amdgcn_raw_buffer_store_b128(ov[i], rsO, vf + i * 4 * (N_ * 2), 0, AUX_NT);
  }
}

extern "C" void kernel_launch(void* const* d_in, const int* in_sizes, int n_in, void* d_out, int out_size, void* d_ws,
                              size_t ws_size, hipStream_t stream) {
  nf4_gemm<<<224, 512, 0, stream>>>((const uint16_t*)d_in[0], (const int*)d_in[1], (const uint16_t*)d_in[2],
                                    (const uint16_t*)d_in[3], (char*)d_ws, (uint16_t*)d_out);
}
```

```cpp
#include <hip/hip_runtime.h>
#include <hip/hip_bf16.h>
#include <stdint.h>

typedef __attribute__((ext_vector_type(8))) short bf16x8;
typedef __attribute__((ext_vector_type(4))) float f32x4;
typedef __attribute__((ext_vector_type(4))) int i32x4;
typedef __attribute__((ext_vector_type(4))) unsigned u32x4;
typedef __attribute__((ext_vector_type(2))) unsigned u32x2;
typedef __attribute__((ext_vector_type(2))) float f32x2;
typedef __attribute__((ext_vector_type(2))) __bf16 bf16x2_t;
typedef __attribute__((address_space(1))) unsigned gu32;

constexpr int M_ = 2048, K_ = 1024, N_ = 3584;
constexpr int BM = 256, BN = 128, BK = 64, NT = K_ / BK;
constexpr int XB = BM * BK * 2;
constexpr int WB = BN * BK * 2;
constexpr int KCB = BN * 16;
constexpr int SB = XB + WB;
constexpr int NS = 3;
constexpr int HDR = 2048;
constexpr int LUT_OFF = -HDR;
constexpr int STG_OFF = SB + XB;
constexpr int SHM_BYTES = HDR + NS * SB;
constexpr size_t WIMG_BYTES = (size_t)28 * NT * WB;
constexpr size_t SYNC_OFF = (size_t)8 << 20;
constexpr unsigned SPIN_MAX = 1u << 17;
constexpr int POLLB_T = 5;
constexpr unsigned CODE_TOUCH_MAX = 11776;
constexpr int AUX_NT = 2, AUX_SC1 = 16;

#define WAIT_V(n) asm volatile("s_waitcnt vmcnt(%0)" ::"n"(n) : "memory")
#define WAIT_L(n) asm volatile("s_waitcnt lgkmcnt(%0)" ::"n"(n) : "memory")
#define WAIT_LB(n) __builtin_amdgcn_s_waitcnt(0xC07F | ((n) << 8))
#define BAR() do { asm volatile("" ::: "memory"); __builtin_amdgcn_s_barrier(); asm volatile("" ::: "memory"); } while (0)
#define SCHED() __builtin_amdgcn_sched_barrier(0)
#define TAG_OF(e) ((e) * 0x9E3779B1u + 0x7F4A7C15u)
#define RLX_AGENT __ATOMIC_RELAXED, __HIP_MEMORY_SCOPE_AGENT

__device__ __forceinline__ unsigned pack_bf16(float a, float b) {
  f32x2 v = {a, b};
  bf16x2_t r = __builtin_convertvector(v, bf16x2_t);
  return __builtin_bit_cast(unsigned, r);
}
__device__ __forceinline__ int kstep_of(int s, int tm, int tn) {
  const int u = (int)(0xFEDCBA9765432180ull >> (4 * s)) & 15;
  return (u & 8) + ((tm + u + tn) & 7);
}

struct QRegs { i32x4 r[8]; };
struct LV { f32x2 v[4][4]; float sc[4]; };
__device__ __forceinline__ f32x2 pair_lookup(const char* lut, int q0, int q1) { return *(const f32x2*)(lut + (q0 << 3) + (q1 << 7)); }
__device__ __forceinline__ u32x4 q_pack(const LV& l, int i) {
  u32x4 o;
  o.x = pack_bf16(l.v[i][0].x * l.sc[i], l.v[i][0].y * l.sc[i]); o.y = pack_bf16(l.v[i][1].x * l.sc[i], l.v[i][1].y * l.sc[i]);
  o.z = pack_bf16(l.v[i][2].x * l.sc[i], l.v[i][2].y * l.sc[i]); o.w = pack_bf16(l.v[i][3].x * l.sc[i], l.v[i][3].y * l.sc[i]);
  return o;
}

__global__ __launch_bounds__(512, 2) void nf4_gemm(const uint16_t* __restrict__ x, const int* __restrict__ qw,
                                                   const uint16_t* __restrict__ cb, const uint16_t* __restrict__ am,
                                                   char* __restrict__ ws, uint16_t* __restrict__ out) {
  __shared__ __attribute__((aligned(1024))) char shm_raw[SHM_BYTES];
  char* const shm = shm_raw + HDR;
  unsigned long long pc0; asm volatile("s_getpc_b64 %0" : "=s"(pc0));
  unsigned ct;
  asm volatile("global_load_dword %0, %1, %2" : "=&v"(ct) : "v"(min((unsigned)(threadIdx.x * 128u), CODE_TOUCH_MAX)), "s"(pc0) : "memory");
  const int bid = blockIdx.x;
  const int xcd = bid & 7, bi = bid >> 3;
  const int hi_ = bi >= 14, tn = (xcd >> 2) * 14 + bi - 14 * hi_, tm = (xcd & 3) * 2 + hi_;
  const int rot = (tm + tn) & 7;
  const int tid = threadIdx.x, lane = tid & 63;
  const int wid = __builtin_amdgcn_readfirstlane(tid >> 6);
  const int nc = lane & 31;
  const int kc0 = wid < 4 ? 2 * wid : 4 * (wid & 1);
  const unsigned qvoff = (unsigned)(((kc0 + (lane >> 5)) * 8 * N_ + nc * 4) * 4);
  const unsigned avoff = (unsigned)(nc * 8);
  const unsigned qs0 = (unsigned)(tn * BN * 4);
  { unsigned t0_ = qvoff, t1_ = avoff; asm volatile("" : "+v"(t0_), "+v"(t1_)); }
  asm volatile("" :: "s"(qs0 + (unsigned)rot * (BK * N_ * 4)));
  SCHED();
  asm volatile("" :: "s"(x), "s"(qw), "s"(cb), "s"(am), "s"(ws), "s"(out));
  gu32* flags = (gu32*)(ws + SYNC_OFF + (size_t)tn * 512);
  char* lut = shm + LUT_OFF;
  const __amdgpu_buffer_rsrc_t rsW = __builtin_amdgcn_make_buffer_rsrc((void*)ws, (short)0, (int)WIMG_BYTES, 0x00020000);
  const __amdgpu_buffer_rsrc_t rsQ = __builtin_amdgcn_make_buffer_rsrc((void*)qw, (short)0, K_ * N_ * 4, 0x00020000);
  const uint16_t* am0 = am + tn * BN;
  const unsigned zero = 0;
#define Q_ROW(Q, j, so_) asm volatile("buffer_load_dwordx4 %0, %1, %2, %3 offen nt" : "=&v"(Q.r[j]) : "v"(qvoff), "s"(rsQ), "s"((so_) + (j) * (N_ * 4)) : "memory")
#define Q_ROWS(Q, so_) do { _Pragma("unroll") for (int j_ = 0; j_ < 8; j_++) Q_ROW(Q, j_, so_); } while (0)
#define Q_SC(S, ks) asm volatile("global_load_dwordx2 %0, %1, %2" : "=&v"(S) : "v"(avoff), "s"(am0 + (ks) * N_) : "memory")
#define Q_SO(ks) (qs0 + (unsigned)(ks) * (BK * N_ * 4))
#define TAG_LOAD(T) asm volatile("global_load_dword %0, %1, %2 offset:256 sc1" : "=&v"(T) : "v"(zero), "s"(flags) : "memory")
#define Q_PIN(q) asm volatile("" : "+v"(q.r[0]), "+v"(q.r[1]), "+v"(q.r[2]), "+v"(q.r[3]), "+v"(q.r[4]), "+v"(q.r[5]), "+v"(q.r[6]), "+v"(q.r[7]))
#define SCALES(S) do { asm volatile("" : "+v"(S));                                                                                \
      lv.sc[0] = __builtin_bit_cast(float, S.x << 16); lv.sc[1] = __builtin_bit_cast(float, S.x & 0xffff0000u);                   \
      lv.sc[2] = __builtin_bit_cast(float, S.y << 16); lv.sc[3] = __builtin_bit_cast(float, S.y & 0xffff0000u); } while (0)
#define ROUND(Q, dst) do { Q_PIN(Q);                                                                                              \
      _Pragma("unroll") for (int i_ = 0; i_ < 4; i_++) _Pragma("unroll") for (int a_ = 0; a_ < 4; a_++)                           \
        lv.v[i_][a_] = pair_lookup(lut, Q.r[2 * a_][i_], Q.r[2 * a_ + 1][i_]);                                                    \
      SCHED();                                                                                                                    \
      char* d_ = shm + (dst) + io;                                                                             \
      _Pragma("unroll") for (int i_ = 0; i_ < 4; i_++) *(u32x4*)(d_ + ((i_ << 4) ^ isw)) = q_pack(lv, i_);                        \
      SCHED(); } while (0)
#define PUBLISH(src, ks, kcp) do { WAIT_L(0); {                                                                    \
        const unsigned g_ = (unsigned)((tn * NT + (ks)) * WB + (kcp) * (2 * KCB) + lane * 16); u32x4 pv_[4];                      \
        _Pragma("unroll") for (int i_ = 0; i_ < 4; i_++) pv_[i_] = *(const u32x4*)(shm + (src) + lane * 16 + i_ * 1024);           \
        _Pragma("unroll") for (int i_ = 0; i_ < 4; i_++) __builtin_amdgcn_raw_buffer_store_b128(pv_[i_], rsW, g_ + i_ * 1024, 0, AUX_SC1); } \
      SCHED(); } while (0)
  LV lv;

  if (__builtin_expect(wid < 4, 1)) {
    QRegs q; u32x2 qsv; unsigned c0, c1, tagv;
    asm volatile("global_load_ushort %0, %2, %4\n\tglobal_load_ushort %1, %3, %4" : "=&v"(c0), "=&v"(c1)
                 : "v"((unsigned)((lane & 15) * 2)), "v"((unsigned)((wid * 4 + (lane >> 4)) * 2)), "s"(cb) : "memory");
    Q_ROWS(q, Q_SO(rot));
    Q_SC(qsv, rot);
    TAG_LOAD(tagv);
    SCHED();
    BAR();
    const int p0 = 32 * (nc >> 3) + 16 * (nc & 1) + 4 * ((nc & 7) >> 1);
    const int io = (lane >> 5) * KCB + p0 * 16, isw = ((p0 >> 3) & 3) << 4;
    f32x4 acc[4][8];
#pragma unroll
    for (int m = 0; m < 4; m++)
#pragma unroll
      for (int n = 0; n < 8; n++) { acc[m][n] = (f32x4){0.f, 0.f, 0.f, 0.f}; asm volatile("" : "+v"(acc[m][n])); }
    WAIT_V(10);
    asm volatile("" : "+v"(c0), "+v"(c1)); asm volatile("" :: "v"(ct));
    *(u32x2*)(lut + tid * 8) = (u32x2){c0 << 16, c1 << 16};
    WAIT_L(0);
    BAR();
    WAIT_V(0);
    SCALES(qsv);
    ROUND(q, XB + kc0 * KCB);
    PUBLISH(XB + kc0 * KCB, rot, wid);
    BAR();
    SCHED();
    const int fr = lane & 15, fq = lane >> 4;
    const int xo0 = (wid * 64 + fr) * 128 + ((fq ^ (fr >> 1)) << 4), xo1 = xo0 ^ 64;
    const int woe = XB + fq * KCB + ((fr ^ (fr >> 3)) << 4), woo = woe ^ 32;
    bf16x8 xa[4], wa[8], xb[4], wb[8];
    typedef __attribute__((address_space(3))) const bf16x8 lds_frag;
    typedef __attribute__((address_space(3))) u32x4 lds_u4;
#define LD(a_) (*(lds_frag*)(a_))
#define MF(m_, n_, X, W) acc[m_][n_] = __builtin_amdgcn_mfma_f32_16x16x32_bf16(W, X, acc[m_][n_], 0, 0, 0)
#define RW(wf, n_, E, O, off) wf[n_] = LD((((n_) & 1) ? (O) : (E)) + (off) + (n_) * 256)
#define VADD(d, v_, s_) asm volatile("v_add_u32 %0, %1, %2" : "=v"(d) : "s"(s_), "v"(v_))
    const unsigned lb = (unsigned)(size_t)shm;
    unsigned bE = lb + woe, bO = lb + woo, bA = lb + xo0, bB = lb + xo1;
    unsigned cE = bE + 2 * SB, cO = bO + 2 * SB, cA = bA + 2 * SB, cB = bB + 2 * SB;
    const unsigned ew = lb + SB + wid * 8192 + fr * 256 + ((fq ^ (fr & 7)) << 4);
    unsigned ew0 = ew, ew1 = ew ^ 64;
    unsigned vf = (unsigned)(((tm * BM + wid * 64 + 32 + fq) * N_ + tn * BN) * 2 + fr * 16);
    unsigned er0 = lb + SB + wid * 8192 + 4096 + fq * 256 + ((fr ^ fq) << 4), er1 = er0 ^ 64;
    asm volatile("" : "+v"(bE), "+v"(bO), "+v"(bA), "+v"(bB), "+v"(cE), "+v"(cO), "+v"(cA), "+v"(cB));
    asm volatile("" : "+v"(ew0), "+v"(ew1), "+v"(vf), "+v"(er0), "+v"(er1));
    WAIT_V(0);
    asm volatile("" : "+v"(tagv));
    if (lane == 0) __hip_atomic_store(flags + rot * 4 + wid, TAG_OF(tagv), RLX_AGENT);
    unsigned aE = bE, aO = bO, aA = bA, aB = bB;
    asm volatile("" : "+v"(aE), "+v"(aO), "+v"(aA), "+v"(aB));
    unsigned so_ = 0;
    SCHED();
    BAR();
    constexpr int T0 = 0;
#pragma unroll
    for (int n = 0; n < 8; n++) RW(wa, n, aE, aO, 0);
    xa[0] = LD(aA);
    SCHED();
#pragma unroll
    for (int m = 1; m < 4; m++) xa[m] = LD(aA + m * 2048);
    SCHED();
    asm volatile(".p2align 5\n\t.rept %0\n\ts_nop 0\n\t.endr" :: "n"(6));
    SCHED();
    WAIT_LB(3);
    SCHED();
#pragma unroll 1
    for (int t = T0; t < NT - 3;) {
#pragma unroll
      for (int i = 0; i < 32; i++) {
        if (i == 31) { WAIT_LB(0); SCHED(); }
        MF(i >> 3, i & 7, xa[i >> 3], wa[i & 7]);
        SCHED();
        if (!(i & 1) && i < 16) RW(wb, i >> 1, aE, aO, 4 * KCB);
        if (!(i & 1) && i >= 16 && i < 24) xb[(i - 16) >> 1] = LD(aB + ((i - 16) >> 1) * 2048);
        if (i == 24) { so_ = so_ == 2 * SB ? 0 : so_ + SB; asm volatile("" : "+s"(so_)); }
        if (i == 25) VADD(aE, bE, so_);
        if (i == 26) VADD(aO, bO, so_);
        if (i == 27) VADD(aA, bA, so_);
        if (i == 28) VADD(aB, bB, so_);
        SCHED();
      }
      BAR();
      SCHED();
#pragma unroll
      for (int i = 0; i < 32; i++) {
        if (i == 31) { WAIT_LB(0); SCHED(); }
        MF(i >> 3, i & 7, xb[i >> 3], wb[i & 7]);
        SCHED();
        if (i == 26) { t++; asm volatile("" : "+s"(t)); }
        if (!(i & 1) && i < 16) RW(wa, i >> 1, aE, aO, 0);
        if (!(i & 1) && i >= 16 && i < 24) xa[(i - 16) >> 1] = LD(aA + ((i - 16) >> 1) * 2048);
        SCHED();
      }
    }
#define PH(m0, XC, WC, XN, WN, xaddr, wE, wO, woff, EX, w14) do { _Pragma("unroll") for (int i = 0; i < 16; i++) {                \
      MF((m0) + (i >> 3), i & 7, XC[i >> 3], WC[i & 7]); SCHED();                                                                 \
      if (i == 5) WAIT_LB(5);                                                                                                     \
      if (i == 14) WAIT_LB(w14);                                                                                                  \
      EX##_D(i); SCHED();                                                                                                         \
      if (i == 0) XN[0] = LD(xaddr);                                                                                              \
      if (i >= 1 && i < 9) RW(WN, i - 1, wE, wO, woff);                                                                           \
      if (i == 9) XN[1] = LD((xaddr) + 2048);                                                                                     \
      SCHED(); EX##_V(i); SCHED(); } } while (0)
    u32x4 ev[4], ov[4];
    bf16x8 wc[8], xc[2];
    unsigned va[8];
#define PK(m, j) ev[((j) >> 2) & 3][(j) & 3] = pack_bf16(acc[(m) + ((j) >> 4)][((j) >> 1) & 7][2 * ((j) & 1)], acc[(m) + ((j) >> 4)][((j) >> 1) & 7][2 * ((j) & 1) + 1])
#define PKS(m, tab, i) do { _Pragma("unroll") for (int j_ = tab[i]; j_ < tab[(i) + 1]; j_++) PK(m, j_); } while (0)
    constexpr int pkE[17] = {0, 1, 2, 3, 4, 5, 6, 7, 8, 9, 10, 11, 12, 13, 15, 16, 16};
    constexpr int pkL[33] = {0, 0, 0, 0, 0, 0, 0, 0, 0, 2, 4, 5, 7, 8, 8, 8, 8, 10, 12, 13, 15, 16, 16, 16, 16, 17, 18, 20, 21, 22, 23, 24, 24};
#define WR(P, off) *(lds_u4*)((((P) & 1) ? ew1 : ew0) + (off) + ((P) >> 1) * 128) = ev[P]
#define NOX_D(i)
#define NOX_V(i)
#define EP_D(i, off) do { if (i >= 10 && i <= 12) WR(i - 10, off); if (i == 15) WR(3, off); } while (0)
#define EP_V(i, m) PKS(m, pkE, i)
#define B1_D(i) EP_D(i, 0)
#define B1_V(i) EP_V(i, 0)
#define B2_D(i) EP_D(i, SB)
#define B2_V(i) EP_V(i, 1)
#define B3_D(i) do { if (i == 6) { BAR(); } if (i >= 10 && i < 14) RW(wc, i - 10, bE, bO, 4 * KCB); } while (0)
#define B3_V(i) do { if (i >= 8) { asm volatile("v_add_u32 %0, %1, %2" : "=v"(va[i - 8]) : "n"((i - 8) * 4 * (N_ * 2)), "v"(vf)); } } while (0)
#define B4_D(i) do { if (i == 10) xc[0] = LD(bB + 4096); if (i >= 11 && i < 15) RW(wc, i - 7, bE, bO, 4 * KCB);                   \
      if (i == 15) xc[1] = LD(bB + 6144); } while (0)
#define B4_V(i)
    const __amdgpu_buffer_rsrc_t rsO = __builtin_amdgcn_make_buffer_rsrc((void*)out, (short)0, M_ * N_ * 2, 0x00020000);
    SCHED();
    PH(0, xa, wa, xb, wb, aB, aE, aO, 4 * KCB, NOX, 2);
    BAR();
    SCHED();
    PH(0, xb, wb, xa, wa, cA, cE, cO, 0, NOX, 2);
    PH(0, xa, wa, xb, wb, cB, cE, cO, 4 * KCB, NOX, 2);
    BAR();
    SCHED();
    PH(0, xb, wb, xa, wa, bA, bE, bO, 0, NOX, 2);
    PH(0, xa, wa, xb, wb, bB, bE, bO, 4 * KCB, NOX, 2);
    PH(0, xb, wb, xa, wa, aA + 4096, aE, aO, 0, NOX, 2);
    PH(2, xa, wa, xb, wb, aB + 4096, aE, aO, 4 * KCB, B1, 5);
    PH(2, xb, wb, xa, wa, cA + 4096, cE, cO, 0, B2, 5);
    PH(2, xa, wa, xb, wb, cB + 4096, cE, cO, 4 * KCB, B3, 6);
    PH(2, xb, wb, xa, wa, bA + 4096, bE, bO, 0, B4, 6);
#define MF15(m, i) MF(m, ((i) & 3) + ((i) >> 3) * 4, (((i) & 4) ? xc[(m) - 2] : xa[(m) - 2]),                                     \
                      (((i) & 4) ? wc[((i) & 3) + ((i) >> 3) * 4] : wa[((i) & 3) + ((i) >> 3) * 4]))
#define OUT_RD(j, off) ov[j] = *(lds_u4*)((((j) & 1) ? er1 : er0) + (off) + (j) * 1024)
#define OUT_ST(j, k) __builtin_amdgcn_raw_buffer_store_b128(ov[j], rsO, va[k], 0, AUX_NT)
#pragma unroll
    for (int i = 0; i < 16; i++) {
      MF15(2, i); SCHED();
      if (i == 2) WAIT_LB(5);
      if (i == 6) WAIT_LB(0);
      if (i == 10) WR(0, 4096);
      if (i == 13) WR(1, 4096);
      SCHED();
      PKS(2, pkL, i);
      SCHED();
    }
#pragma unroll
    for (int i = 0; i < 16; i++) {
      MF15(3, i); SCHED();
      if (i == 2) WR(2, 4096);
      if (i == 5) WR(3, 4096);
      if (i >= 6 && i < 10) OUT_RD(i - 6, 0);
      if (i == 11) WR(0, SB + 4096);
      if (i == 12) WAIT_LB(3);
      if (i == 14) WAIT_LB(1);
      if (i >= 12) OUT_ST(i - 12, i - 12);
      SCHED();
      PKS(2, pkL, 16 + i);
      SCHED();
    }
    WR(1, SB + 4096);
    SCHED();
#pragma unroll
    for (int j = 8; j < 16; j++) PK(3, j);
    SCHED();
    WR(2, SB + 4096); WR(3, SB + 4096);
    SCHED();
#pragma unroll
    for (int j = 0; j < 4; j++) OUT_RD(j, SB);
    SCHED();
#pragma unroll
    for (int j = 0; j < 4; j++) OUT_ST(j, 4 + j);
  } else {
  if (wid < 6) {
    const int h = wid - 4;
    unsigned xv[16];
#pragma unroll
    for (int i = 0; i < 16; i++) xv[i] = (unsigned)((8 * i + (lane >> 3)) * (K_ * 2) + (((lane & 7) ^ ((i & 1) * 4 + (lane >> 4))) << 4));
    const char* xbase = (const char*)(x + (size_t)(tm * BM + h * 128) * K_);
    const unsigned ldsx = (unsigned)(size_t)(shm) + h * 16384;
#define X8(o, l_, b_)                                                                                                             \
      asm volatile("s_mov_b32 m0, %8\n\ts_nop 0\n\tglobal_load_lds_dwordx4 %0, %9\n\t"                                            \
                   "s_add_u32 m0, m0, 0x400\n\ts_nop 0\n\tglobal_load_lds_dwordx4 %1, %9\n\t"                                     \
                   "s_add_u32 m0, m0, 0x400\n\ts_nop 0\n\tglobal_load_lds_dwordx4 %2, %9\n\t"                                     \
                   "s_add_u32 m0, m0, 0x400\n\ts_nop 0\n\tglobal_load_lds_dwordx4 %3, %9\n\t"                                     \
                   "s_add_u32 m0, m0, 0x400\n\ts_nop 0\n\tglobal_load_lds_dwordx4 %4, %9\n\t"                                     \
                   "s_add_u32 m0, m0, 0x400\n\ts_nop 0\n\tglobal_load_lds_dwordx4 %5, %9\n\t"                                     \
                   "s_add_u32 m0, m0, 0x400\n\ts_nop 0\n\tglobal_load_lds_dwordx4 %6, %9\n\t"                                     \
                   "s_add_u32 m0, m0, 0x400\n\ts_nop 0\n\tglobal_load_lds_dwordx4 %7, %9"                                         \
                   :: "v"(xv[o]), "v"(xv[o + 1]), "v"(xv[o + 2]), "v"(xv[o + 3]), "v"(xv[o + 4]), "v"(xv[o + 5]), "v"(xv[o + 6]), "v"(xv[o + 7]), \
                      "s"(l_), "s"(b_) : "memory", "scc")
#define ISSUE_X(s, slot) do { const char* b_ = xbase + kstep_of(s, tm, tn) * 128; const unsigned l_ = ldsx + (slot) * SB;       \
      X8(0, l_, b_); X8(8, l_ + 8192, b_); } while (0)
    BAR();
    BAR();
    BAR();
    ISSUE_X(0, 0);
    ISSUE_X(1, 1);
    WAIT_V(16);
    asm volatile("" :: "v"(ct));
    BAR();
    int xslot = 2;
#pragma unroll 1
    for (int t = -1; t < NT - 2; t++) {
      if (t + 3 < NT) { ISSUE_X(t + 3, xslot); xslot = xslot == NS - 1 ? 0 : xslot + 1; WAIT_V(16); } else { WAIT_V(0); }
      BAR();
    }
  } else {
    QRegs qa, qb; u32x2 sb; unsigned tagv;
    constexpr unsigned RO = 16 * N_ * 4;
    Q_SC(sb, rot + 8); TAG_LOAD(tagv);
    const int p0 = 32 * (nc >> 3) + 16 * (nc & 1) + 4 * ((nc & 7) >> 1);
    const int io = (lane >> 5) * KCB + p0 * 16, isw = ((p0 >> 3) & 3) << 4;
    BAR();
    Q_ROWS(qa, Q_SO(rot + 8));
    Q_ROWS(qb, Q_SO(rot + 8) + RO);
    BAR();
    BAR();
    WAIT_V(8);
    asm volatile("" :: "v"(ct));
    SCALES(sb);
    ROUND(qa, STG_OFF + kc0 * KCB);
    WAIT_V(0);
    const unsigned fvo = (unsigned)(lane * 4);
    unsigned fv = tagv;
    ROUND(qb, STG_OFF + (kc0 + 2) * KCB);
    WAIT_L(0);
    asm volatile("" : "+v"(tagv));
    const unsigned tag = TAG_OF(tagv);
   if (wid == 6) {
    const char* wbase = ws + (size_t)tn * NT * WB;
    const unsigned wv = (unsigned)(lane * 16), wv2 = wv + 4096, wv3 = wv + 8192, wv4 = wv + 12288;
    const unsigned ldsw = (unsigned)(size_t)(shm) + XB;
#define ISSUE_W(s, slot) do { const char* b_ = wbase + (size_t)kstep_of(s, tm, tn) * WB; const unsigned l_ = ldsw + (slot) * SB; \
      asm volatile("s_mov_b32 m0, %4\n\ts_nop 0\n\t"                                                                              \
                   "global_load_lds_dwordx4 %0, %5 sc1\n\tglobal_load_lds_dwordx4 %0, %5 offset:1024 sc1\n\t"                     \
                   "global_load_lds_dwordx4 %0, %5 offset:2048 sc1\n\tglobal_load_lds_dwordx4 %0, %5 offset:3072 sc1\n\t"         \
                   "s_add_u32 m0, m0, 0x1000\n\ts_nop 0\n\t"                                                                      \
                   "global_load_lds_dwordx4 %1, %5 sc1\n\tglobal_load_lds_dwordx4 %1, %5 offset:1024 sc1\n\t"                     \
                   "global_load_lds_dwordx4 %1, %5 offset:2048 sc1\n\tglobal_load_lds_dwordx4 %1, %5 offset:3072 sc1\n\t"         \
                   "s_add_u32 m0, m0, 0x1000\n\ts_nop 0\n\t"                                                                      \
                   "global_load_lds_dwordx4 %2, %5 sc1\n\tglobal_load_lds_dwordx4 %2, %5 offset:1024 sc1\n\t"                     \
                   "global_load_lds_dwordx4 %2, %5 offset:2048 sc1\n\tglobal_load_lds_dwordx4 %2, %5 offset:3072 sc1\n\t"         \
                   "s_add_u32 m0, m0, 0x1000\n\ts_nop 0\n\t"                                                                      \
                   "global_load_lds_dwordx4 %3, %5 sc1\n\tglobal_load_lds_dwordx4 %3, %5 offset:1024 sc1\n\t"                     \
                   "global_load_lds_dwordx4 %3, %5 offset:2048 sc1\n\tglobal_load_lds_dwordx4 %3, %5 offset:3072 sc1"             \
                   :: "v"(wv), "v"(wv2), "v"(wv3), "v"(wv4), "s"(l_), "s"(b_) : "memory", "scc"); } while (0)
#define SET_RDY(f_) do { const unsigned long long b_ = __ballot((f_) == tag);                                                     \
      rda = (unsigned)__builtin_amdgcn_readfirstlane((unsigned)b_); rdb = (unsigned)__builtin_amdgcn_readfirstlane((unsigned)(b_ >> 32)); } while (0)
#define POLL_BLOCK() do { unsigned f_; asm volatile("global_load_dword %0, %1, %2 sc1\n\ts_waitcnt vmcnt(0)" : "=&v"(f_) : "v"(fvo), "s"(flags) : "memory"); \
      SET_RDY(f_); } while (0)
#define READY(s) (((((s) > 8 ? rdb : rda) >> ((kstep_of(s, tm, tn) & 7) * 4)) & 15u) == 15u)
#define NEXT_W() do { nw++; wslot = wslot == NS - 1 ? 0 : wslot + 1; } while (0)
    unsigned rda = 0, rdb = 0, spins = 0;
    BAR();
    int nw = 2, wslot = 2;
    bool ppoll = false, preg = false;
#pragma unroll 1
    for (int t = -1; t < NT - 2; t++) {
      const int lim = t + 3 < NT ? t + 3 : NT - 1;
      if (preg) { WAIT_V(16); } else { WAIT_V(0); }
      asm volatile("" : "+v"(fv));
      if (ppoll) SET_RDY(fv);
      if (nw <= t + 2) {
#pragma unroll 1
        while (!READY(nw)) { if (++spins > SPIN_MAX) { rda = ~0u; rdb = ~0u; break; } POLL_BLOCK(); }
        ISSUE_W(nw, wslot); NEXT_W();
      }
      ppoll = spins <= SPIN_MAX && (t < POLLB_T ? rda != ~0u : rdb != ~0u);
      asm volatile("global_load_dword %0, %1, %2 sc1" : "+v"(fv) : "v"(fvo), "s"(ppoll ? (const char*)flags : (const char*)am) : "memory");
      preg = nw <= lim && READY(nw);
      if (preg) { ISSUE_W(nw, wslot); NEXT_W(); WAIT_V(17); } else { WAIT_V(1); }
      BAR();
    }
    WAIT_V(0);
    asm volatile("" : "+v"(fv));
    if (tm == 0) {
#pragma unroll 1
      while ((rda & rdb) != ~0u && ++spins < SPIN_MAX) POLL_BLOCK();
      if (lane == 0) __hip_atomic_store(flags + 64, tag, RLX_AGENT);
    }
   } else {
    BAR();
    u32x4 bv[16];
#pragma unroll
    for (int i = 0; i < 16; i++) bv[i] = *(const u32x4*)(shm + STG_OFF + lane * 16 + i * 1024);
    WAIT_L(0);
    SCHED();
    BAR();
    const unsigned g_ = (unsigned)((tn * NT + rot + 8) * WB + lane * 16);
#pragma unroll
    for (int i = 0; i < 8; i++) __builtin_amdgcn_raw_buffer_store_b128(bv[i], rsW, g_ + i * 1024, 0, AUX_SC1);
    SCHED();
    BAR();
#pragma unroll
    for (int i = 8; i < 16; i++) __builtin_amdgcn_raw_buffer_store_b128(bv[i], rsW, g_ + i * 1024, 0, AUX_SC1);
    SCHED();
    BAR();
    BAR();
    WAIT_V(0);
    asm volatile("" :: "v"(fv));
    if (lane < 4) __hip_atomic_store(flags + (rot + 8) * 4 + lane, tag, RLX_AGENT);
#pragma unroll 1
    for (int t = 4; t < NT - 1; t++) { BAR(); }
   }
  }
  {
    const __amdgpu_buffer_rsrc_t rsO = __builtin_amdgcn_make_buffer_rsrc((void*)out, (short)0, M_ * N_ * 2, 0x00020000);
    typedef __attribute__((address_space(3))) u32x4 lds_u4;
    const int r4 = lane >> 4, c = lane & 15;
    unsigned e0 = (unsigned)(size_t)shm + SB + (wid & 3) * 8192 + r4 * 256 + ((c ^ r4) << 4), e1 = e0 ^ 64;
    unsigned va[8];
#pragma unroll
    for (int i = 0; i < 8; i++) { va[i] = (unsigned)(((tm * BM + (wid & 3) * 64 + r4 + 4 * i) * N_ + tn * BN) * 2 + c * 16); asm volatile("" : "+v"(va[i])); }
    asm volatile("" : "+v"(e0), "+v"(e1));
    asm volatile("" :: "s"(rsO));
    SCHED();
    BAR();
    SCHED();
    u32x4 ov[8];
#pragma unroll
    for (int i = 0; i < 8; i++) { ov[i] = *(lds_u4*)(((i & 1) ? e1 : e0) + (i >> 2) * SB + (i & 3) * 1024); SCHED(); }
#pragma unroll
    for (int i = 0; i < 8; i++) __builtin_amdgcn_raw_buffer_store_b128(ov[i], rsO, va[i], 0, AUX_NT);
  }
  }
  asm volatile("s_nop 0" ::: "memory");
}

extern "C" void kernel_launch(void* const* d_in, const int* in_sizes, int n_in, void* d_out, int out_size, void* d_ws,
                              size_t ws_size, hipStream_t stream) {
  nf4_gemm<<<224, 512, 0, stream>>>((const uint16_t*)d_in[0], (const int*)d_in[1], (const uint16_t*)d_in[2],
                                    (const uint16_t*)d_in[3], (char*)d_ws, (uint16_t*)d_out);
}
```

```cpp
#include <hip/hip_runtime.h>
#include <hip/hip_bf16.h>
#include <stdint.h>

typedef __attribute__((ext_vector_type(8))) short bf16x8;
typedef __attribute__((ext_vector_type(4))) float f32x4;
typedef __attribute__((ext_vector_type(4))) int i32x4;
typedef __attribute__((ext_vector_type(4))) unsigned u32x4;
typedef __attribute__((ext_vector_type(2))) unsigned u32x2;
typedef __attribute__((ext_vector_type(2))) float f32x2;
typedef __attribute__((ext_vector_type(2))) __bf16 bf16x2_t;
typedef __attribute__((address_space(1))) unsigned gu32;

constexpr int M_ = 2048, K_ = 1024, N_ = 3584;
constexpr int BM = 256, BN = 128, BK = 64, NT = K_ / BK;
constexpr int XB = BM * BK * 2;
constexpr int WB = BN * BK * 2;
constexpr int KCB = BN * 16;
constexpr int SB = XB + WB;
constexpr int NS = 3;
constexpr int HDR = 2048;
constexpr int LUT_OFF = -HDR;
constexpr int STG_OFF = SB + XB;
constexpr int SHM_BYTES = HDR + NS * SB;
constexpr size_t WIMG_BYTES = (size_t)28 * NT * WB;
constexpr size_t SYNC_OFF = (size_t)8 << 20;
constexpr unsigned SPIN_MAX = 1u << 17;
constexpr int POLLB_T = 5;
constexpr unsigned CODE_TOUCH_MAX = 11776;
constexpr int AUX_NT = 2, AUX_SC1 = 16;

#define WAIT_V(n) asm volatile("s_waitcnt vmcnt(%0)" ::"n"(n) : "memory")
#define WAIT_L(n) asm volatile("s_waitcnt lgkmcnt(%0)" ::"n"(n) : "memory")
#define WAIT_LB(n) __builtin_amdgcn_s_waitcnt(0xC07F | ((n) << 8))
#define BAR() do { asm volatile("" ::: "memory"); __builtin_amdgcn_s_barrier(); asm volatile("" ::: "memory"); } while (0)
#define SCHED() __builtin_amdgcn_sched_barrier(0)
#define TAG_OF(e) ((e) * 0x9E3779B1u + 0x7F4A7C15u)
#define RLX_AGENT __ATOMIC_RELAXED, __HIP_MEMORY_SCOPE_AGENT

__device__ __forceinline__ unsigned pack_bf16(float a, float b) {
  f32x2 v = {a, b};
  bf16x2_t r = __builtin_convertvector(v, bf16x2_t);
  return __builtin_bit_cast(unsigned, r);
}
__device__ __forceinline__ int kstep_of(int s, int tm, int tn) {
  const int u = (int)(0xFEDCBA9765432180ull >> (4 * s)) & 15;
  return (u & 8) + ((tm + u) & 7);
}

struct QRegs { i32x4 r[8]; };
struct LV { f32x2 v[4][4]; float sc[4]; };
__device__ __forceinline__ f32x2 pair_lookup(const char* lut, int q0, int q1) { return *(const f32x2*)(lut + (q0 << 3) + (q1 << 7)); }
__device__ __forceinline__ u32x4 q_pack(const LV& l, int i) {
  u32x4 o;
  o.x = pack_bf16(l.v[i][0].x * l.sc[i], l.v[i][0].y * l.sc[i]); o.y = pack_bf16(l.v[i][1].x * l.sc[i], l.v[i][1].y * l.sc[i]);
  o.z = pack_bf16(l.v[i][2].x * l.sc[i], l.v[i][2].y * l.sc[i]); o.w = pack_bf16(l.v[i][3].x * l.sc[i], l.v[i][3].y * l.sc[i]);
  return o;
}

__global__ __launch_bounds__(512, 2) void nf4_gemm(const uint16_t* __restrict__ x, const int* __restrict__ qw,
                                                   const uint16_t* __restrict__ cb, const uint16_t* __restrict__ am,
                                                   char* __restrict__ ws, uint16_t* __restrict__ out) {
  __shared__ __attribute__((aligned(1024))) char shm_raw[SHM_BYTES];
  char* const shm = shm_raw + HDR;
  unsigned long long pc0; asm volatile("s_getpc_b64 %0" : "=s"(pc0));
  unsigned ct;
  asm volatile("global_load_dword %0, %1, %2" : "=&v"(ct) : "v"(min((unsigned)(threadIdx.x * 128u), CODE_TOUCH_MAX)), "s"(pc0) : "memory");
#define PWAIT ""
  {
    unsigned long long xp; unsigned pt, pu, po, ps, pa, pb, pc;
    asm volatile("s_cmp_gt_u32 %8, 31\n\ts_cbranch_scc1 9f\n\ts_load_dwordx2 %0, %9, 0\n\t"
                 "s_lshl_b32 %1, %8, 1\n\ts_and_b32 %1, %1, 6\n\ts_bfe_u32 %2, %8, 0x10003\n\ts_or_b32 %1, %1, %2\n\t"
                 "v_lshlrev_b32 %5, 10, %10\n\tv_and_b32 %5, 0xfffff800, %5\n\tv_and_b32 %6, 1, %10\n\tv_lshl_or_b32 %5, %6, 6, %5\n\t"
                 "v_lshl_or_b32 %5, %1, 19, %5\n\t"
                 "s_mov_b32 %3, 0xfdb96428\n\ts_cmp_lt_u32 %8, 16\n\ts_cmov_b32 %3, 0xeca75310\n\ts_mov_b32 %4, %12\n\ts_waitcnt lgkmcnt(0)\n"
                 "1:\n\ts_add_u32 %2, %3, %1\n\ts_xor_b32 %2, %2, %3\n\ts_and_b32 %2, %2, 7\n\ts_xor_b32 %2, %2, %3\n\ts_and_b32 %2, %2, 15\n\t"
                 "v_lshl_add_u32 %6, %2, 7, %5\n\tglobal_load_dword %7, %6, %0\n"
                 "2:\n\ts_sleep %11\n\ts_sub_u32 %4, %4, 1\n\ts_cbranch_scc0 2b\n\ts_mov_b32 %4, 0\n\t" PWAIT
                 "s_lshr_b32 %3, %3, 4\n\ts_cbranch_scc1 1b\n\ts_waitcnt vmcnt(0)\n\ts_endpgm\n9:"
                 : "=&s"(xp), "=&s"(pt), "=&s"(pu), "=&s"(po), "=&s"(ps), "=&v"(pa), "=&v"(pb), "=&v"(pc)
                 : "s"(blockIdx.x), "s"(__builtin_amdgcn_kernarg_segment_ptr()), "v"(threadIdx.x), "n"(12), "n"(70 / 12 - 1), "v"(ct) : "memory", "scc");
  }
  const int bid = blockIdx.x - 32;
  const int xcd = bid & 7, bi = bid >> 3;
  const int hi_ = bi >= 14, tn = (xcd >> 2) * 14 + bi - 14 * hi_, tm = (xcd & 3) * 2 + hi_;
  const int rot = tm;
  const int tid = threadIdx.x, lane = tid & 63;
  const int wid = __builtin_amdgcn_readfirstlane(tid >> 6);
  const int nc = lane & 31;
  const int kc0 = wid < 4 ? 2 * wid : 4 * (wid & 1);
  const unsigned qvoff = (unsigned)(((kc0 + (lane >> 5)) * 8 * N_ + nc * 4) * 4);
  const unsigned avoff = (unsigned)(nc * 8);
  const unsigned qs0 = (unsigned)(tn * BN * 4);
  { unsigned t0_ = qvoff, t1_ = avoff; asm volatile("" : "+v"(t0_), "+v"(t1_)); }
  asm volatile("" :: "s"(qs0 + (unsigned)rot * (BK * N_ * 4)));
  SCHED();
  asm volatile("" :: "s"(x), "s"(qw), "s"(cb), "s"(am), "s"(ws), "s"(out));
  gu32* flags = (gu32*)(ws + SYNC_OFF + (size_t)tn * 512);
  char* lut = shm + LUT_OFF;
  const __amdgpu_buffer_rsrc_t rsW = __builtin_amdgcn_make_buffer_rsrc((void*)ws, (short)0, (int)WIMG_BYTES, 0x00020000);
  const __amdgpu_buffer_rsrc_t rsQ = __builtin_amdgcn_make_buffer_rsrc((void*)qw, (short)0, K_ * N_ * 4, 0x00020000);
  const uint16_t* am0 = am + tn * BN;
  const unsigned zero = 0;
#define Q_ROW(Q, j, so_) asm volatile("buffer_load_dwordx4 %0, %1, %2, %3 offen nt" : "=&v"(Q.r[j]) : "v"(qvoff), "s"(rsQ), "s"((so_) + (j) * (N_ * 4)) : "memory")
#define Q_ROWS(Q, so_) do { _Pragma("unroll") for (int j_ = 0; j_ < 8; j_++) Q_ROW(Q, j_, so_); } while (0)
#define Q_SC(S, ks) asm volatile("global_load_dwordx2 %0, %1, %2" : "=&v"(S) : "v"(avoff), "s"(am0 + (ks) * N_) : "memory")
#define Q_SO(ks) (qs0 + (unsigned)(ks) * (BK * N_ * 4))
#define TAG_LOAD(T) asm volatile("global_load_dword %0, %1, %2 offset:256 sc1" : "=&v"(T) : "v"(zero), "s"(flags) : "memory")
#define Q_PIN(q) asm volatile("" : "+v"(q.r[0]), "+v"(q.r[1]), "+v"(q.r[2]), "+v"(q.r[3]), "+v"(q.r[4]), "+v"(q.r[5]), "+v"(q.r[6]), "+v"(q.r[7]))
#define SCALES(S) do { asm volatile("" : "+v"(S));                                                                                \
      lv.sc[0] = __builtin_bit_cast(float, S.x << 16); lv.sc[1] = __builtin_bit_cast(float, S.x & 0xffff0000u);                   \
      lv.sc[2] = __builtin_bit_cast(float, S.y << 16); lv.sc[3] = __builtin_bit_cast(float, S.y & 0xffff0000u); } while (0)
#define ROUND(Q, dst) do { Q_PIN(Q);                                                                                              \
      _Pragma("unroll") for (int i_ = 0; i_ < 4; i_++) _Pragma("unroll") for (int a_ = 0; a_ < 4; a_++)                           \
        lv.v[i_][a_] = pair_lookup(lut, Q.r[2 * a_][i_], Q.r[2 * a_ + 1][i_]);                                                    \
      SCHED();                                                                                                                    \
      char* d_ = shm + (dst) + io;                                                                             \
      _Pragma("unroll") for (int i_ = 0; i_ < 4; i_++) *(u32x4*)(d_ + ((i_ << 4) ^ isw)) = q_pack(lv, i_);                        \
      SCHED(); } while (0)
#define PUBLISH(src, ks, kcp) do { WAIT_L(0); {                                                                    \
        const unsigned g_ = (unsigned)((tn * NT + (ks)) * WB + (kcp) * (2 * KCB) + lane * 16); u32x4 pv_[4];                      \
        _Pragma("unroll") for (int i_ = 0; i_ < 4; i_++) pv_[i_] = *(const u32x4*)(shm + (src) + lane * 16 + i_ * 1024);           \
        _Pragma("unroll") for (int i_ = 0; i_ < 4; i_++) __builtin_amdgcn_raw_buffer_store_b128(pv_[i_], rsW, g_ + i_ * 1024, 0, AUX_SC1); } \
      SCHED(); } while (0)
  LV lv;

  if (__builtin_expect(wid < 4, 1)) {
    QRegs q; u32x2 qsv; unsigned c0, c1, tagv;
    asm volatile("global_load_ushort %0, %2, %4\n\tglobal_load_ushort %1, %3, %4" : "=&v"(c0), "=&v"(c1)
                 : "v"((unsigned)((lane & 15) * 2)), "v"((unsigned)((wid * 4 + (lane >> 4)) * 2)), "s"(cb) : "memory");
    Q_ROWS(q, Q_SO(rot));
    Q_SC(qsv, rot);
    TAG_LOAD(tagv);
    SCHED();
    BAR();
    const int p0 = 32 * (nc >> 3) + 16 * (nc & 1) + 4 * ((nc & 7) >> 1);
    const int io = (lane >> 5) * KCB + p0 * 16, isw = ((p0 >> 3) & 3) << 4;
    f32x4 acc[4][8];
#pragma unroll
    for (int m = 0; m < 4; m++)
#pragma unroll
      for (int n = 0; n < 8; n++) { acc[m][n] = (f32x4){0.f, 0.f, 0.f, 0.f}; asm volatile("" : "+v"(acc[m][n])); }
    WAIT_V(10);
    asm volatile("" : "+v"(c0), "+v"(c1)); asm volatile("" :: "v"(ct));
    *(u32x2*)(lut + tid * 8) = (u32x2){c0 << 16, c1 << 16};
    WAIT_L(0);
    BAR();
    WAIT_V(0);
    SCALES(qsv);
    ROUND(q, XB + kc0 * KCB);
    PUBLISH(XB + kc0 * KCB, rot, wid);
    BAR();
    SCHED();
    const int fr = lane & 15, fq = lane >> 4;
    const int xo0 = (wid * 64 + fr) * 128 + ((fq ^ (fr >> 1)) << 4), xo1 = xo0 ^ 64;
    const int woe = XB + fq * KCB + ((fr ^ (fr >> 3)) << 4), woo = woe ^ 32;
    bf16x8 xa[4], wa[8], xb[4], wb[8];
    typedef __attribute__((address_space(3))) const bf16x8 lds_frag;
    typedef __attribute__((address_space(3))) u32x4 lds_u4;
#define LD(a_) (*(lds_frag*)(a_))
#define MF(m_, n_, X, W) acc[m_][n_] = __builtin_amdgcn_mfma_f32_16x16x32_bf16(W, X, acc[m_][n_], 0, 0, 0)
#define RW(wf, n_, E, O, off) wf[n_] = LD((((n_) & 1) ? (O) : (E)) + (off) + (n_) * 256)
#define VADD(d, v_, s_) asm volatile("v_add_u32 %0, %1, %2" : "=v"(d) : "s"(s_), "v"(v_))
    const unsigned lb = (unsigned)(size_t)shm;
    unsigned bE = lb + woe, bO = lb + woo, bA = lb + xo0, bB = lb + xo1;
    unsigned cE = bE + 2 * SB, cO = bO + 2 * SB, cA = bA + 2 * SB, cB = bB + 2 * SB;
    const unsigned ew = lb + SB + wid * 8192 + fr * 256 + ((fq ^ (fr & 7)) << 4);
    unsigned ew0 = ew, ew1 = ew ^ 64;
    unsigned vf = (unsigned)(((tm * BM + wid * 64 + 32 + fq) * N_ + tn * BN) * 2 + fr * 16);
    unsigned er0 = lb + SB + wid * 8192 + 4096 + fq * 256 + ((fr ^ fq) << 4), er1 = er0 ^ 64;
    asm volatile("" : "+v"(bE), "+v"(bO), "+v"(bA), "+v"(bB), "+v"(cE), "+v"(cO), "+v"(cA), "+v"(cB));
    asm volatile("" : "+v"(ew0), "+v"(ew1), "+v"(vf), "+v"(er0), "+v"(er1));
    WAIT_V(0);
    asm volatile("" : "+v"(tagv));
    if (lane == 0) __hip_atomic_store(flags + rot * 4 + wid, TAG_OF(tagv), RLX_AGENT);
    unsigned aE = bE, aO = bO, aA = bA, aB = bB;
    asm volatile("" : "+v"(aE), "+v"(aO), "+v"(aA), "+v"(aB));
    unsigned so_ = 0;
    SCHED();
    BAR();
    constexpr int T0 = 0;
#pragma unroll
    for (int n = 0; n < 8; n++) RW(wa, n, aE, aO, 0);
    xa[0] = LD(aA);
    SCHED();
#pragma unroll
    for (int m = 1; m < 4; m++) xa[m] = LD(aA + m * 2048);
    SCHED();
    asm volatile(".p2align 5\n\t.rept %0\n\ts_nop 0\n\t.endr" :: "n"(6));
    SCHED();
    WAIT_LB(3);
    SCHED();
#pragma unroll 1
    for (int t = T0; t < NT - 3;) {
#pragma unroll
      for (int i = 0; i < 32; i++) {
        if (i == 31) { WAIT_LB(0); SCHED(); }
        MF(i >> 3, i & 7, xa[i >> 3], wa[i & 7]);
        SCHED();
        if (!(i & 1) && i < 16) RW(wb, i >> 1, aE, aO, 4 * KCB);
        if (!(i & 1) && i >= 16 && i < 24) xb[(i - 16) >> 1] = LD(aB + ((i - 16) >> 1) * 2048);
        if (i == 24) { so_ = so_ == 2 * SB ? 0 : so_ + SB; asm volatile("" : "+s"(so_)); }
        if (i == 25) VADD(aE, bE, so_);
        if (i == 26) VADD(aO, bO, so_);
        if (i == 27) VADD(aA, bA, so_);
        if (i == 28) VADD(aB, bB, so_);
        SCHED();
      }
      BAR();
      SCHED();
#pragma unroll
      for (int i = 0; i < 32; i++) {
        if (i == 31) { WAIT_LB(0); SCHED(); }
        MF(i >> 3, i & 7, xb[i >> 3], wb[i & 7]);
        SCHED();
        if (i == 26) { t++; asm volatile("" : "+s"(t)); }
        if (!(i & 1) && i < 16) RW(wa, i >> 1, aE, aO, 0);
        if (!(i & 1) && i >= 16 && i < 24) xa[(i - 16) >> 1] = LD(aA + ((i - 16) >> 1) * 2048);
        SCHED();
      }
    }
#define PH(m0, XC, WC, XN, WN, xaddr, wE, wO, woff, EX, w14) do { _Pragma("unroll") for (int i = 0; i < 16; i++) {                \
      MF((m0) + (i >> 3), i & 7, XC[i >> 3], WC[i & 7]); SCHED();                                                                 \
      if (i == 5) WAIT_LB(5);                                                                                                     \
      if (i == 14) WAIT_LB(w14);                                                                                                  \
      EX##_D(i); SCHED();                                                                                                         \
      if (i == 0) XN[0] = LD(xaddr);                                                                                              \
      if (i >= 1 && i < 9) RW(WN, i - 1, wE, wO, woff);                                                                           \
      if (i == 9) XN[1] = LD((xaddr) + 2048);                                                                                     \
      SCHED(); EX##_V(i); SCHED(); } } while (0)
    u32x4 ev[4], ov[4];
    bf16x8 wc[8], xc[2];
    unsigned va[8];
#define PK(m, j) ev[((j) >> 2) & 3][(j) & 3] = pack_bf16(acc[(m) + ((j) >> 4)][((j) >> 1) & 7][2 * ((j) & 1)], acc[(m) + ((j) >> 4)][((j) >> 1) & 7][2 * ((j) & 1) + 1])
#define PKS(m, tab, i) do { _Pragma("unroll") for (int j_ = tab[i]; j_ < tab[(i) + 1]; j_++) PK(m, j_); } while (0)
    constexpr int pkE[17] = {0, 1, 2, 3, 4, 5, 6, 7, 8, 9, 10, 11, 12, 13, 15, 16, 16};
    constexpr int pkL[33] = {0, 0, 0, 0, 0, 0, 0, 0, 0, 2, 4, 5, 7, 8, 8, 8, 8, 10, 12, 13, 15, 16, 16, 16, 16, 17, 18, 20, 21, 22, 23, 24, 24};
#define WR(P, off) *(lds_u4*)((((P) & 1) ? ew1 : ew0) + (off) + ((P) >> 1) * 128) = ev[P]
#define NOX_D(i)
#define NOX_V(i)
#define EP_D(i, off) do { if (i >= 10 && i <= 12) WR(i - 10, off); if (i == 15) WR(3, off); } while (0)
#define EP_V(i, m) PKS(m, pkE, i)
#define B1_D(i) EP_D(i, 0)
#define B1_V(i) EP_V(i, 0)
#define B2_D(i) EP_D(i, SB)
#define B2_V(i) EP_V(i, 1)
#define B3_D(i) do { if (i == 6) { BAR(); } if (i >= 10 && i < 14) RW(wc, i - 10, bE, bO, 4 * KCB); } while (0)
#define B3_V(i) do { if (i >= 8) { asm volatile("v_add_u32 %0, %1, %2" : "=v"(va[i - 8]) : "n"((i - 8) * 4 * (N_ * 2)), "v"(vf)); } } while (0)
#define B4_D(i) do { if (i == 10) xc[0] = LD(bB + 4096); if (i >= 11 && i < 15) RW(wc, i - 7, bE, bO, 4 * KCB);                   \
      if (i == 15) xc[1] = LD(bB + 6144); } while (0)
#define B4_V(i)
    const __amdgpu_buffer_rsrc_t rsO = __builtin_amdgcn_make_buffer_rsrc((void*)out, (short)0, M_ * N_ * 2, 0x00020000);
    SCHED();
    PH(0, xa, wa, xb, wb, aB, aE, aO, 4 * KCB, NOX, 2);
    BAR();
    SCHED();
    PH(0, xb, wb, xa, wa, cA, cE, cO, 0, NOX, 2);
    PH(0, xa, wa, xb, wb, cB, cE, cO, 4 * KCB, NOX, 2);
    BAR();
    SCHED();
    PH(0, xb, wb, xa, wa, bA, bE, bO, 0, NOX, 2);
    PH(0, xa, wa, xb, wb, bB, bE, bO, 4 * KCB, NOX, 2);
    PH(0, xb, wb, xa, wa, aA + 4096, aE, aO, 0, NOX, 2);
    PH(2, xa, wa, xb, wb, aB + 4096, aE, aO, 4 * KCB, B1, 5);
    PH(2, xb, wb, xa, wa, cA + 4096, cE, cO, 0, B2, 5);
    PH(2, xa, wa, xb, wb, cB + 4096, cE, cO, 4 * KCB, B3, 6);
    PH(2, xb, wb, xa, wa, bA + 4096, bE, bO, 0, B4, 6);
#define MF15(m, i) MF(m, ((i) & 3) + ((i) >> 3) * 4, (((i) & 4) ? xc[(m) - 2] : xa[(m) - 2]),                                     \
                      (((i) & 4) ? wc[((i) & 3) + ((i) >> 3) * 4] : wa[((i) & 3) + ((i) >> 3) * 4]))
#define OUT_RD(j, off) ov[j] = *(lds_u4*)((((j) & 1) ? er1 : er0) + (off) + (j) * 1024)
#define OUT_ST(j, k) __builtin_amdgcn_raw_buffer_store_b128(ov[j], rsO, va[k], 0, AUX_NT)
#pragma unroll
    for (int i = 0; i < 16; i++) {
      MF15(2, i); SCHED();
      if (i == 2) WAIT_LB(5);
      if (i == 6) WAIT_LB(0);
      if (i == 10) WR(0, 4096);
      if (i == 13) WR(1, 4096);
      SCHED();
      PKS(2, pkL, i);
      SCHED();
    }
#pragma unroll
    for (int i = 0; i < 16; i++) {
      MF15(3, i); SCHED();
      if (i == 2) WR(2, 4096);
      if (i == 5) WR(3, 4096);
      if (i >= 6 && i < 10) OUT_RD(i - 6, 0);
      if (i == 11) WR(0, SB + 4096);
      if (i == 12) WAIT_LB(3);
      if (i == 14) WAIT_LB(1);
      if (i >= 12) OUT_ST(i - 12, i - 12);
      SCHED();
      PKS(2, pkL, 16 + i);
      SCHED();
    }
    WR(1, SB + 4096);
    SCHED();
#pragma unroll
    for (int j = 8; j < 16; j++) PK(3, j);
    SCHED();
    WR(2, SB + 4096); WR(3, SB + 4096);
    SCHED();
#pragma unroll
    for (int j = 0; j < 4; j++) OUT_RD(j, SB);
    SCHED();
#pragma unroll
    for (int j = 0; j < 4; j++) OUT_ST(j, 4 + j);
  } else {
  if (wid < 6) {
    const int h = wid - 4;
    unsigned xv[16];
#pragma unroll
    for (int i = 0; i < 16; i++) xv[i] = (unsigned)((8 * i + (lane >> 3)) * (K_ * 2) + (((lane & 7) ^ ((i & 1) * 4 + (lane >> 4))) << 4));
    const char* xbase = (const char*)(x + (size_t)(tm * BM + h * 128) * K_);
    const unsigned ldsx = (unsigned)(size_t)(shm) + h * 16384;
#define X8(o, l_, b_)                                                                                                             \
      asm volatile("s_mov_b32 m0, %8\n\ts_nop 0\n\tglobal_load_lds_dwordx4 %0, %9\n\t"                                            \
                   "s_add_u32 m0, m0, 0x400\n\ts_nop 0\n\tglobal_load_lds_dwordx4 %1, %9\n\t"                                     \
                   "s_add_u32 m0, m0, 0x400\n\ts_nop 0\n\tglobal_load_lds_dwordx4 %2, %9\n\t"                                     \
                   "s_add_u32 m0, m0, 0x400\n\ts_nop 0\n\tglobal_load_lds_dwordx4 %3, %9\n\t"                                     \
                   "s_add_u32 m0, m0, 0x400\n\ts_nop 0\n\tglobal_load_lds_dwordx4 %4, %9\n\t"                                     \
                   "s_add_u32 m0, m0, 0x400\n\ts_nop 0\n\tglobal_load_lds_dwordx4 %5, %9\n\t"                                     \
                   "s_add_u32 m0, m0, 0x400\n\ts_nop 0\n\tglobal_load_lds_dwordx4 %6, %9\n\t"                                     \
                   "s_add_u32 m0, m0, 0x400\n\ts_nop 0\n\tglobal_load_lds_dwordx4 %7, %9"                                         \
                   :: "v"(xv[o]), "v"(xv[o + 1]), "v"(xv[o + 2]), "v"(xv[o + 3]), "v"(xv[o + 4]), "v"(xv[o + 5]), "v"(xv[o + 6]), "v"(xv[o + 7]), \
                      "s"(l_), "s"(b_) : "memory", "scc")
#define ISSUE_X(s, slot) do { const char* b_ = xbase + kstep_of(s, tm, tn) * 128; const unsigned l_ = ldsx + (slot) * SB;       \
      X8(0, l_, b_); X8(8, l_ + 8192, b_); } while (0)
    BAR();
    BAR();
    BAR();
    ISSUE_X(0, 0);
    ISSUE_X(1, 1);
    WAIT_V(16);
    asm volatile("" :: "v"(ct));
    BAR();
    int xslot = 2;
#pragma unroll 1
    for (int t = -1; t < NT - 2; t++) {
      if (t + 3 < NT) { ISSUE_X(t + 3, xslot); xslot = xslot == NS - 1 ? 0 : xslot + 1; WAIT_V(16); } else { WAIT_V(0); }
      BAR();
    }
  } else {
    QRegs qa, qb; u32x2 sb; unsigned tagv;
    constexpr unsigned RO = 16 * N_ * 4;
    Q_SC(sb, rot + 8); TAG_LOAD(tagv);
    const int p0 = 32 * (nc >> 3) + 16 * (nc & 1) + 4 * ((nc & 7) >> 1);
    const int io = (lane >> 5) * KCB + p0 * 16, isw = ((p0 >> 3) & 3) << 4;
    BAR();
    Q_ROWS(qa, Q_SO(rot + 8));
    Q_ROWS(qb, Q_SO(rot + 8) + RO);
    BAR();
    BAR();
    WAIT_V(8);
    asm volatile("" :: "v"(ct));
    SCALES(sb);
    ROUND(qa, STG_OFF + kc0 * KCB);
    WAIT_V(0);
    const unsigned fvo = (unsigned)(lane * 4);
    unsigned fv = tagv;
    ROUND(qb, STG_OFF + (kc0 + 2) * KCB);
    WAIT_L(0);
    asm volatile("" : "+v"(tagv));
    const unsigned tag = TAG_OF(tagv);
   if (wid == 6) {
    __builtin_amdgcn_s_setprio(3);
    const char* wbase = ws + (size_t)tn * NT * WB;
    const unsigned wv = (unsigned)(lane * 16), wv2 = wv + 4096, wv3 = wv + 8192, wv4 = wv + 12288;
    const unsigned ldsw = (unsigned)(size_t)(shm) + XB;
#define ISSUE_W(s, slot) do { const char* b_ = wbase + (size_t)kstep_of(s, tm, tn) * WB; const unsigned l_ = ldsw + (slot) * SB; \
      asm volatile("s_mov_b32 m0, %4\n\ts_nop 0\n\t"                                                                              \
                   "global_load_lds_dwordx4 %0, %5 sc1\n\tglobal_load_lds_dwordx4 %0, %5 offset:1024 sc1\n\t"                     \
                   "global_load_lds_dwordx4 %0, %5 offset:2048 sc1\n\tglobal_load_lds_dwordx4 %0, %5 offset:3072 sc1\n\t"         \
                   "s_add_u32 m0, m0, 0x1000\n\ts_nop 0\n\t"                                                                      \
                   "global_load_lds_dwordx4 %1, %5 sc1\n\tglobal_load_lds_dwordx4 %1, %5 offset:1024 sc1\n\t"                     \
                   "global_load_lds_dwordx4 %1, %5 offset:2048 sc1\n\tglobal_load_lds_dwordx4 %1, %5 offset:3072 sc1\n\t"         \
                   "s_add_u32 m0, m0, 0x1000\n\ts_nop 0\n\t"                                                                      \
                   "global_load_lds_dwordx4 %2, %5 sc1\n\tglobal_load_lds_dwordx4 %2, %5 offset:1024 sc1\n\t"                     \
                   "global_load_lds_dwordx4 %2, %5 offset:2048 sc1\n\tglobal_load_lds_dwordx4 %2, %5 offset:3072 sc1\n\t"         \
                   "s_add_u32 m0, m0, 0x1000\n\ts_nop 0\n\t"                                                                      \
                   "global_load_lds_dwordx4 %3, %5 sc1\n\tglobal_load_lds_dwordx4 %3, %5 offset:1024 sc1\n\t"                     \
                   "global_load_lds_dwordx4 %3, %5 offset:2048 sc1\n\tglobal_load_lds_dwordx4 %3, %5 offset:3072 sc1"             \
                   :: "v"(wv), "v"(wv2), "v"(wv3), "v"(wv4), "s"(l_), "s"(b_) : "memory", "scc"); } while (0)
#define SET_RDY(f_) do { const unsigned long long b_ = __ballot((f_) == tag);                                                     \
      rda = (unsigned)__builtin_amdgcn_readfirstlane((unsigned)b_); rdb = (unsigned)__builtin_amdgcn_readfirstlane((unsigned)(b_ >> 32)); } while (0)
#define POLL_BLOCK() do { unsigned f_; asm volatile("global_load_dword %0, %1, %2 sc1\n\ts_waitcnt vmcnt(0)" : "=&v"(f_) : "v"(fvo), "s"(flags) : "memory"); \
      SET_RDY(f_); } while (0)
#define READY(s) (((((s) > 8 ? rdb : rda) >> ((kstep_of(s, tm, tn) & 7) * 4)) & 15u) == 15u)
#define NEXT_W() do { nw++; wslot = wslot == NS - 1 ? 0 : wslot + 1; } while (0)
    unsigned rda = 0, rdb = 0, spins = 0;
    BAR();
    int nw = 2, wslot = 2;
    bool ppoll = false, preg = false;
#pragma unroll 1
    for (int t = -1; t < NT - 2; t++) {
      const int lim = t + 3 < NT ? t + 3 : NT - 1;
      if (preg) { WAIT_V(16); } else { WAIT_V(0); }
      asm volatile("" : "+v"(fv));
      if (ppoll) SET_RDY(fv);
      if (nw <= t + 2) {
#pragma unroll 1
        while (!READY(nw)) { if (++spins > SPIN_MAX) { rda = ~0u; rdb = ~0u; break; } POLL_BLOCK(); }
        ISSUE_W(nw, wslot); NEXT_W();
      }
      ppoll = spins <= SPIN_MAX && (t < POLLB_T ? rda != ~0u : rdb != ~0u);
      asm volatile("global_load_dword %0, %1, %2 sc1" : "+v"(fv) : "v"(fvo), "s"(ppoll ? (const char*)flags : (const char*)am) : "memory");
      preg = nw <= lim && READY(nw);
      if (preg) { ISSUE_W(nw, wslot); NEXT_W(); WAIT_V(17); } else { WAIT_V(1); }
      BAR();
    }
    WAIT_V(0);
    asm volatile("" : "+v"(fv));
    if (tm == 0) {
#pragma unroll 1
      while ((rda & rdb) != ~0u && ++spins < SPIN_MAX) POLL_BLOCK();
      if (lane == 0) __hip_atomic_store(flags + 64, tag, RLX_AGENT);
    }
   } else {
    BAR();
    u32x4 bv[16];
#pragma unroll
    for (int i = 0; i < 16; i++) bv[i] = *(const u32x4*)(shm + STG_OFF + lane * 16 + i * 1024);
    WAIT_L(0);
    SCHED();
    BAR();
    const unsigned g_ = (unsigned)((tn * NT + rot + 8) * WB + lane * 16);
#pragma unroll
    for (int i = 0; i < 8; i++) __builtin_amdgcn_raw_buffer_store_b128(bv[i], rsW, g_ + i * 1024, 0, AUX_SC1);
    SCHED();
    BAR();
#pragma unroll
    for (int i = 8; i < 16; i++) __builtin_amdgcn_raw_buffer_store_b128(bv[i], rsW, g_ + i * 1024, 0, AUX_SC1);
    SCHED();
    BAR();
    BAR();
    WAIT_V(0);
    asm volatile("" :: "v"(fv));
    if (lane < 4) __hip_atomic_store(flags + (rot + 8) * 4 + lane, tag, RLX_AGENT);
#pragma unroll 1
    for (int t = 4; t < NT - 1; t++) { BAR(); }
   }
  }
  {
    const __amdgpu_buffer_rsrc_t rsO = __builtin_amdgcn_make_buffer_rsrc((void*)out, (short)0, M_ * N_ * 2, 0x00020000);
    typedef __attribute__((address_space(3))) u32x4 lds_u4;
    const int r4 = lane >> 4, c = lane & 15;
    unsigned e0 = (unsigned)(size_t)shm + SB + (wid & 3) * 8192 + r4 * 256 + ((c ^ r4) << 4), e1 = e0 ^ 64;
    unsigned va[8];
#pragma unroll
    for (int i = 0; i < 8; i++) { va[i] = (unsigned)(((tm * BM + (wid & 3) * 64 + r4 + 4 * i) * N_ + tn * BN) * 2 + c * 16); asm volatile("" : "+v"(va[i])); }
    asm volatile("" : "+v"(e0), "+v"(e1));
    asm volatile("" :: "s"(rsO));
    SCHED();
    BAR();
    SCHED();
    u32x4 ov[8];
#pragma unroll
    for (int i = 0; i < 8; i++) { ov[i] = *(lds_u4*)(((i & 1) ? e1 : e0) + (i >> 2) * SB + (i & 3) * 1024); SCHED(); }
#pragma unroll
    for (int i = 0; i < 8; i++) __builtin_amdgcn_raw_buffer_store_b128(ov[i], rsO, va[i], 0, AUX_NT);
  }
  }
  asm volatile("s_nop 0" ::: "memory");
}

extern "C" void kernel_launch(void* const* d_in, const int* in_sizes, int n_in, void* d_out, int out_size, void* d_ws,
                              size_t ws_size, hipStream_t stream) {
  constexpr int grid = 256;
  nf4_gemm<<<grid, 512, 0, stream>>>((const uint16_t*)d_in[0], (const int*)d_in[1], (const uint16_t*)d_in[2],
                                    (const uint16_t*)d_in[3], (char*)d_ws, (uint16_t*)d_out);
}
```

```cpp
#include <hip/hip_runtime.h>
#include <hip/hip_bf16.h>
#include <stdint.h>

typedef __attribute__((ext_vector_type(8))) short bf16x8;
typedef __attribute__((ext_vector_type(4))) float f32x4;
typedef __attribute__((ext_vector_type(4))) int i32x4;
typedef __attribute__((ext_vector_type(4))) unsigned u32x4;
typedef __attribute__((ext_vector_type(2))) unsigned u32x2;
typedef __attribute__((ext_vector_type(2))) float f32x2;
typedef __attribute__((ext_vector_type(2))) __bf16 bf16x2_t;
typedef __attribute__((address_space(1))) unsigned gu32;

constexpr int M_ = 2048, K_ = 1024, N_ = 3584;
constexpr int BM = 256, BN = 128, BK = 64, NT = K_ / BK;
constexpr int XB = BM * BK * 2;
constexpr int WB = BN * BK * 2;
constexpr int KCB = BN * 16;
constexpr int SB = XB + WB;
constexpr int NS = 3;
constexpr int HDR = 2048;
constexpr int LUT_OFF = -HDR;
constexpr int STG_OFF = SB + XB;
constexpr int SHM_BYTES = HDR + NS * SB;
constexpr size_t WIMG_BYTES = (size_t)28 * NT * WB;
constexpr size_t SYNC_OFF = (size_t)8 << 20;
constexpr unsigned SPIN_MAX = 1u << 17;
constexpr int POLLB_T = 5;
constexpr unsigned CODE_TOUCH_MAX = 11008;
constexpr int AUX_NT = 2, AUX_SC1 = 16;

#define WAIT_V(n) asm volatile("s_waitcnt vmcnt(%0)" ::"n"(n) : "memory")
#define WAIT_L(n) asm volatile("s_waitcnt lgkmcnt(%0)" ::"n"(n) : "memory")
#define WAIT_LB(n) __builtin_amdgcn_s_waitcnt(0xC07F | ((n) << 8))
#define BAR() do { asm volatile("" ::: "memory"); __builtin_amdgcn_s_barrier(); asm volatile("" ::: "memory"); } while (0)
#define SCHED() __builtin_amdgcn_sched_barrier(0)
#define TAG_OF(e) ((e) * 0x9E3779B1u + 0x7F4A7C15u)
#define RLX_AGENT __ATOMIC_RELAXED, __HIP_MEMORY_SCOPE_AGENT

__device__ __forceinline__ unsigned pack_bf16(float a, float b) {
  f32x2 v = {a, b};
  bf16x2_t r = __builtin_convertvector(v, bf16x2_t);
  return __builtin_bit_cast(unsigned, r);
}
__device__ __forceinline__ int kstep_of(int s, int tm, int tn) {
  const int u = (int)(0xFEDCBA9765432180ull >> (4 * s)) & 15;
  return (u & 8) + ((tm + u) & 7);
}

__device__ __forceinline__ void pin_acc(f32x4& a, int k) {
  switch (k) {
    case 0: asm volatile("" : "+{v[2:5]}"(a)); break;
    case 1: asm volatile("" : "+{v[6:9]}"(a)); break;
    case 2: asm volatile("" : "+{v[10:13]}"(a)); break;
    case 3: asm volatile("" : "+{v[14:17]}"(a)); break;
    case 4: asm volatile("" : "+{v[18:21]}"(a)); break;
    case 5: asm volatile("" : "+{v[22:25]}"(a)); break;
    case 6: asm volatile("" : "+{v[26:29]}"(a)); break;
    case 7: asm volatile("" : "+{v[30:33]}"(a)); break;
    case 8: asm volatile("" : "+{v[34:37]}"(a)); break;
    case 9: asm volatile("" : "+{v[38:41]}"(a)); break;
    case 10: asm volatile("" : "+{v[42:45]}"(a)); break;
    case 11: asm volatile("" : "+{v[46:49]}"(a)); break;
    case 12: asm volatile("" : "+{v[50:53]}"(a)); break;
    case 13: asm volatile("" : "+{v[54:57]}"(a)); break;
    case 14: asm volatile("" : "+{v[58:61]}"(a)); break;
    case 15: asm volatile("" : "+{v[62:65]}"(a)); break;
    case 16: asm volatile("" : "+{v[66:69]}"(a)); break;
    case 17: asm volatile("" : "+{v[70:73]}"(a)); break;
    case 18: asm volatile("" : "+{v[74:77]}"(a)); break;
    case 19: asm volatile("" : "+{v[78:81]}"(a)); break;
    case 20: asm volatile("" : "+{v[82:85]}"(a)); break;
    case 21: asm volatile("" : "+{v[86:89]}"(a)); break;
    case 22: asm volatile("" : "+{v[90:93]}"(a)); break;
    case 23: asm volatile("" : "+{v[94:97]}"(a)); break;
    case 24: asm volatile("" : "+{v[98:101]}"(a)); break;
    case 25: asm volatile("" : "+{v[102:105]}"(a)); break;
    case 26: asm volatile("" : "+{v[106:109]}"(a)); break;
    case 27: asm volatile("" : "+{v[110:113]}"(a)); break;
    case 28: asm volatile("" : "+{v[114:117]}"(a)); break;
    case 29: asm volatile("" : "+{v[118:121]}"(a)); break;
    case 30: asm volatile("" : "+{v[122:125]}"(a)); break;
    case 31: asm volatile("" : "+{v[126:129]}"(a)); break;
  }
}
struct TailPlan {
  int st[160], pk[161], pj[64], pd[64];
  constexpr TailPlan() : st(), pk(), pj(), pd() {
    for (int k = 0; k < 64; k++) { pj[k] = k < 48 ? k >> 1 : 24 + ((k - 48) >> 3) * 4 + (k & 3); pd[k] = k < 48 ? k & 1 : (k >> 2) & 1; }
    const char* const plan =
        "................" "................" "................" "......1....2t2.." "s11.s.11s11.t1.." "s11.s.11s11.t1.." "s11.s.11s11.t1.." "s11.s.1.s...2t11" "t1.t1.t1.t1.t22t" "..t21s..s..t21s.";
    int k = 0, j = 0;
    for (int u = 0; u < 160; u++) {
      const char c = plan[u];
      pk[u] = k; st[u] = (c == 's' || c == 't') ? j++ : -1;
      k += c == '2' ? 2 : (c == '1' || c == 't') ? 1 : 0;
    }
    pk[160] = k;
  }
};
constexpr TailPlan tl;
static_assert(tl.pk[160] == 60, "");
struct QRegs { i32x4 r[8]; };
struct LV { f32x2 v[4][4]; float sc[4]; };
__device__ __forceinline__ f32x2 pair_lookup(const char* lut, int q0, int q1) { return *(const f32x2*)(lut + (q0 << 3) + (q1 << 7)); }
__device__ __forceinline__ u32x4 q_pack(const LV& l, int i) {
  u32x4 o;
  o.x = pack_bf16(l.v[i][0].x * l.sc[i], l.v[i][0].y * l.sc[i]); o.y = pack_bf16(l.v[i][1].x * l.sc[i], l.v[i][1].y * l.sc[i]);
  o.z = pack_bf16(l.v[i][2].x * l.sc[i], l.v[i][2].y * l.sc[i]); o.w = pack_bf16(l.v[i][3].x * l.sc[i], l.v[i][3].y * l.sc[i]);
  return o;
}

__global__ __launch_bounds__(512, 2) void nf4_gemm(const uint16_t* __restrict__ x, const int* __restrict__ qw,
                                                   const uint16_t* __restrict__ cb, const uint16_t* __restrict__ am,
                                                   char* __restrict__ ws, uint16_t* __restrict__ out) {
  __shared__ __attribute__((aligned(1024))) char shm_raw[SHM_BYTES];
  char* const shm = shm_raw + HDR;
  unsigned long long pc0; asm volatile("s_getpc_b64 %0" : "=s"(pc0));
  unsigned ct;
  asm volatile("global_load_dword %0, %1, %2" : "=&v"(ct) : "v"(min((unsigned)(threadIdx.x * 128u), CODE_TOUCH_MAX)), "s"(pc0) : "memory");
#define PWAIT ""
  {
    unsigned long long xp; unsigned pt, pu, po, ps, pa, pb, pc;
    asm volatile("s_cmp_gt_u32 %8, 31\n\ts_cbranch_scc1 9f\n\ts_load_dwordx2 %0, %9, 0\n\t"
                 "s_lshl_b32 %1, %8, 1\n\ts_and_b32 %1, %1, 6\n\ts_bfe_u32 %2, %8, 0x10003\n\ts_or_b32 %1, %1, %2\n\t"
                 "v_lshlrev_b32 %5, 10, %10\n\tv_and_b32 %5, 0xfffff800, %5\n\tv_and_b32 %6, 1, %10\n\tv_lshl_or_b32 %5, %6, 6, %5\n\t"
                 "v_lshl_or_b32 %5, %1, 19, %5\n\t"
                 "s_mov_b32 %3, 0xfdb96428\n\ts_cmp_lt_u32 %8, 16\n\ts_cmov_b32 %3, 0xeca75310\n\ts_mov_b32 %4, %12\n\ts_waitcnt lgkmcnt(0)\n"
                 "1:\n\ts_add_u32 %2, %3, %1\n\ts_xor_b32 %2, %2, %3\n\ts_and_b32 %2, %2, 7\n\ts_xor_b32 %2, %2, %3\n\ts_and_b32 %2, %2, 15\n\t"
                 "v_lshl_add_u32 %6, %2, 7, %5\n\tglobal_load_dword %7, %6, %0\n"
                 "2:\n\ts_sleep %11\n\ts_sub_u32 %4, %4, 1\n\ts_cbranch_scc0 2b\n\ts_mov_b32 %4, 0\n\t" PWAIT
                 "s_lshr_b32 %3, %3, 4\n\ts_cbranch_scc1 1b\n\ts_waitcnt vmcnt(0)\n\ts_endpgm\n9:"
                 : "=&s"(xp), "=&s"(pt), "=&s"(pu), "=&s"(po), "=&s"(ps), "=&v"(pa), "=&v"(pb), "=&v"(pc)
                 : "s"(blockIdx.x), "s"(__builtin_amdgcn_kernarg_segment_ptr()), "v"(threadIdx.x), "n"(12), "n"(70 / 12 - 1), "v"(ct) : "memory", "scc");
  }
  const int bid = blockIdx.x - 32;
  const int xcd = bid & 7, bi = bid >> 3;
  const int hi_ = bi >= 14, tn = (xcd >> 2) * 14 + bi - 14 * hi_, tm = (xcd & 3) * 2 + hi_;
  const int rot = tm;
  const int tid = threadIdx.x, lane = tid & 63;
  const int wid = __builtin_amdgcn_readfirstlane(tid >> 6);
  const int nc = lane & 31;
  const int kc0 = wid < 4 ? 2 * wid : 4 * (wid & 1);
  const unsigned qvoff = (unsigned)(((kc0 + (lane >> 5)) * 8 * N_ + nc * 4) * 4);
  const unsigned avoff = (unsigned)(nc * 8);
  const unsigned qs0 = (unsigned)(tn * BN * 4);
  { unsigned t0_ = qvoff, t1_ = avoff; asm volatile("" : "+v"(t0_), "+v"(t1_)); }
  asm volatile("" :: "s"(qs0 + (unsigned)rot * (BK * N_ * 4)));
  SCHED();
  asm volatile("" :: "s"(x), "s"(qw), "s"(cb), "s"(am), "s"(ws), "s"(out));
  gu32* flags = (gu32*)(ws + SYNC_OFF + (size_t)tn * 512);
  char* lut = shm + LUT_OFF;
  const __amdgpu_buffer_rsrc_t rsW = __builtin_amdgcn_make_buffer_rsrc((void*)ws, (short)0, (int)WIMG_BYTES, 0x00020000);
  const __amdgpu_buffer_rsrc_t rsQ = __builtin_amdgcn_make_buffer_rsrc((void*)qw, (short)0, K_ * N_ * 4, 0x00020000);
  const uint16_t* am0 = am + tn * BN;
  const unsigned zero = 0;
#define Q_ROW(Q, j, so_) asm volatile("buffer_load_dwordx4 %0, %1, %2, %3 offen nt" : "=&v"(Q.r[j]) : "v"(qvoff), "s"(rsQ), "s"((so_) + (j) * (N_ * 4)) : "memory")
#define Q_ROWS(Q, so_) do { _Pragma("unroll") for (int j_ = 0; j_ < 8; j_++) Q_ROW(Q, j_, so_); } while (0)
#define Q_SC(S, ks) asm volatile("global_load_dwordx2 %0, %1, %2" : "=&v"(S) : "v"(avoff), "s"(am0 + (ks) * N_) : "memory")
#define Q_SO(ks) (qs0 + (unsigned)(ks) * (BK * N_ * 4))
#define TAG_LOAD(T) asm volatile("global_load_dword %0, %1, %2 offset:256 sc1" : "=&v"(T) : "v"(zero), "s"(flags) : "memory")
#define Q_PIN(q) asm volatile("" : "+v"(q.r[0]), "+v"(q.r[1]), "+v"(q.r[2]), "+v"(q.r[3]), "+v"(q.r[4]), "+v"(q.r[5]), "+v"(q.r[6]), "+v"(q.r[7]))
#define SCALES(S) do { asm volatile("" : "+v"(S));                                                                                \
      lv.sc[0] = __builtin_bit_cast(float, S.x << 16); lv.sc[1] = __builtin_bit_cast(float, S.x & 0xffff0000u);                   \
      lv.sc[2] = __builtin_bit_cast(float, S.y << 16); lv.sc[3] = __builtin_bit_cast(float, S.y & 0xffff0000u); } while (0)
#define ROUND(Q, dst, PUB, g_) do { Q_PIN(Q);                                                                                     \
      _Pragma("unroll") for (int i_ = 0; i_ < 4; i_++) _Pragma("unroll") for (int a_ = 0; a_ < 4; a_++)                           \
        lv.v[i_][a_] = pair_lookup(lut, Q.r[2 * a_][i_], Q.r[2 * a_ + 1][i_]);                                                    \
      SCHED();                                                                                                                    \
      char* d_ = shm + (dst) + io;                                                                                                \
      _Pragma("unroll") for (int i_ = 0; i_ < 4; i_++) { const u32x4 o_ = q_pack(lv, i_);                                         \
        if (PUB) { __builtin_amdgcn_raw_buffer_store_b128(o_, rsW, (g_) + i_ * 256, 0, AUX_SC1); SCHED(); }                       \
        *(u32x4*)(d_ + i_ * 256) = o_; }                                                                                          \
      SCHED(); } while (0)
  LV lv;

  if (__builtin_expect(wid < 4, 1)) {
    QRegs q; u32x2 qsv; unsigned c0, c1, tagv;
    asm volatile("global_load_ushort %0, %2, %4\n\tglobal_load_ushort %1, %3, %4" : "=&v"(c0), "=&v"(c1)
                 : "v"((unsigned)((lane & 15) * 2)), "v"((unsigned)((wid * 4 + (lane >> 4)) * 2)), "s"(cb) : "memory");
    Q_ROWS(q, Q_SO(rot));
    Q_SC(qsv, rot);
    TAG_LOAD(tagv);
    SCHED();
    BAR();
    const int io = (lane >> 4) * 1024 + (lane & 15) * 16;
    unsigned gA = (unsigned)((tn * NT + rot) * WB + kc0 * KCB + io);
    asm volatile("" : "+v"(gA));
    f32x4 acc[4][8];
#pragma unroll
    for (int m = 0; m < 4; m++)
#pragma unroll
      for (int n = 0; n < 8; n++) { acc[m][n] = (f32x4){0.f, 0.f, 0.f, 0.f}; pin_acc(acc[m][n], m * 8 + n); }
    WAIT_V(10);
    asm volatile("" : "+v"(c0), "+v"(c1)); asm volatile("" :: "v"(ct));
    *(u32x2*)(lut + tid * 8) = (u32x2){c0 << 16, c1 << 16};
    WAIT_L(0);
    BAR();
    WAIT_V(0);
    SCALES(qsv);
    ROUND(q, XB + kc0 * KCB, 1, gA);
    BAR();
    SCHED();
    const int fr = lane & 15, fq = lane >> 4;
    const int xo0 = (wid * 64 + fr) * 128 + ((fq ^ (fr >> 1)) << 4), xo1 = xo0 ^ 64;
    const int wo = XB + fq * KCB + (fr << 4);
    bf16x8 xl[4], xh[4], wa[8], wb[8];
    typedef __attribute__((address_space(3))) const bf16x8 lds_frag;
#define LD(a_) (*(lds_frag*)(a_))
#define MF(m_, n_, X, W) do { acc[m_][n_] = __builtin_amdgcn_mfma_f32_16x16x32_bf16(X, W, acc[m_][n_], 0, 0, 0); pin_acc(acc[m_][n_], (m_) * 8 + (n_)); } while (0)
#define RW(wf, j_, W_, cb) wf[j_] = LD((W_) + ((j_) & 1) * (4 * KCB) + ((cb) * 4 + ((j_) >> 1)) * 256)
#define RX(xf, j_, A, B, off) xf[j_] = LD((((j_) & 1) ? (B) : (A)) + (off) + ((j_) >> 1) * 2048)
#define MP(m0, cb, i, X, W) MF((m0) + (((i) >> 3) & 1), (cb) * 4 + (((i) >> 1) & 3), X[(((i) >> 2) & 2) + ((i) & 1)], W[(i) & 7])
#define VADD(d, v_, s_) asm volatile("v_add_u32 %0, %1, %2" : "=v"(d) : "s"(s_), "v"(v_))
    const unsigned lb = (unsigned)(size_t)shm;
    unsigned bW = lb + wo, bA = lb + xo0, bB = lb + xo1;
    unsigned vf = (unsigned)(((tm * BM + wid * 64 + 4 * fq) * N_ + tn * BN) * 2 + fr * 8);
    asm volatile("" : "+v"(bW), "+v"(bA), "+v"(bB), "+v"(vf));
    __amdgpu_buffer_rsrc_t rsO = __builtin_amdgcn_make_buffer_rsrc((void*)out, (short)0, M_ * N_ * 2, 0x00020000);
    asm volatile("" : "+s"(rsO));
    unsigned ro[16];
#pragma unroll
    for (int j = 0; j < 16; j++) { ro[j] = (unsigned)(((j >> 2) * 16 + (j & 3)) * (N_ * 2)); asm volatile("" : "+s"(ro[j])); }
    WAIT_V(0);
    asm volatile("" : "+v"(tagv));
    if (lane == 0) __hip_atomic_store(flags + rot * 4 + wid, TAG_OF(tagv), RLX_AGENT);
    unsigned aW = bW, aA = bA, aB = bB;
    asm volatile("" : "+v"(aW), "+v"(aA), "+v"(aB));
    unsigned so_ = 0;
    WAIT_L(0);
    SCHED();
    BAR();
    RX(xl, 0, aA, aB, 0);
    SCHED();
    RW(wa, 0, aW, 0);
    SCHED();
    RX(xl, 1, aA, aB, 0);
    SCHED();
#pragma unroll
    for (int n = 1; n < 8; n++) { RW(wa, n, aW, 0); SCHED(); }
    RX(xl, 2, aA, aB, 0); RX(xl, 3, aA, aB, 0);
    SCHED();
    RX(xh, 0, aA, aB, 4096); RX(xh, 1, aA, aB, 4096);
    SCHED();
    asm volatile(".p2align 5\n\t.rept %0\n\ts_nop 0\n\t.endr" :: "n"(7));
    SCHED();
#define HALF0(EX) do { _Pragma("unroll") for (int i = 0; i < 32; i++) {                                                            \
        if (i < 16) MP(0, 0, i, xl, wa); else MP(2, 0, i, xh, wa);                                                                \
        SCHED();                                                                                                                  \
        if (i == 7) WAIT_LB(4);                                                                                                   \
        if (i == 15) WAIT_LB(6);                                                                                                  \
        if (i == 0 || i == 2) RX(xh, 2 + (i >> 1), aA, aB, 4096);                                                                 \
        if (!(i & 1) && i >= 4 && i < 20) RW(wb, (i - 4) >> 1, aW, 1);                                                            \
        if (i == 20) { so_ = so_ == 2 * SB ? 0 : so_ + SB; asm volatile("" : "+s"(so_)); }                                        \
        if (i == 23) VADD(aW, bW, so_);                                                                                           \
        if (i == 25) VADD(aA, bA, so_);                                                                                           \
        if (i == 27) VADD(aB, bB, so_);                                                                                           \
        if (i == 26) { EX; }                                                                                                      \
        if (i == 28) WAIT_LB(0);                                                                                            \
        if (i == 29) BAR();                                                      \
        SCHED();                                                                                                                  \
      } } while (0)
    HALF0((void)0);
#pragma unroll 1
    for (int t = 0; t < NT - 3;) {
#pragma unroll
      for (int i = 0; i < 32; i++) {
        if (i == 31) { WAIT_LB(4); SCHED(); }
        if (i < 16) MP(0, 1, i, xl, wb); else MP(2, 1, i, xh, wb);
        SCHED();
        if (!(i & 1) && i < 8) RW(wa, i >> 1, aW, 0);
        if (i == 8 || i == 10) RX(xl, (i - 8) >> 1, aA, aB, 0);
        if (!(i & 1) && i >= 12 && i < 20) RW(wa, (i - 4) >> 1, aW, 0);
        if (i == 20 || i == 22) RX(xl, (i - 16) >> 1, aA, aB, 0);
        if (i == 24 || i == 26) RX(xh, (i - 24) >> 1, aA, aB, 4096);
        SCHED();
      }
      HALF0(t++; asm volatile("" : "+s"(t)));
    }
    u32x2 ev[32];
    bf16x8 wc[8], xc[4];
#define PC_M(j) (((j) >> 4) * 2 + (((j) >> 2) & 1))
#define PC_C(j) ((((j) >> 3) ^ ((j) >> 4)) & 1)
#define PK(j, d) ev[j][d] = pack_bf16(acc[PC_M(j)][PC_C(j) * 4 + 2 * (d)][(j) & 3], acc[PC_M(j)][PC_C(j) * 4 + 2 * (d) + 1][(j) & 3])
#define OUT_ST(j) __builtin_amdgcn_raw_buffer_store_b64(ev[j], rsO, vf + PC_C(j) * 128, ro[PC_M(j) * 4 + ((j) & 3)], AUX_NT)
#define PH(p, m0, cb, XC, WC, RD) do { _Pragma("unroll") for (int i = 0; i < 16; i++) {                                           \
      MP(m0, cb, i, XC, WC); SCHED();                                                                                             \
      OUTW(p * 16 + i, RD(i)); } } while (0)
#define OUTW(u, R_) do {                                                                                                          \
      if (tl.st[u] >= 0) { OUT_ST(tl.st[u] & 31); SCHED(); }                                                                      \
      R_; SCHED();                                                                                                                \
      if (tl.pk[(u) + 1] > tl.pk[u]) PK(tl.pj[tl.pk[u]], tl.pd[tl.pk[u]]);                                                        \
      if (tl.pk[(u) + 1] > tl.pk[u] + 1) PK(tl.pj[tl.pk[u] + 1], tl.pd[tl.pk[u] + 1]);                                            \
      SCHED();                                                                                                                    \
      if (tl.st[u] >= 0) asm volatile("" :: "v"(ev[tl.st[u] & 31])); } while (0)
#define RDS(i, WN, W_, cb, XN, xA, xB, xo, x0, nx0, nx1, w14) do {                                                                \
      if (i == 5) WAIT_LB(5);                                                                                                     \
      if (i == 14) WAIT_LB(w14);                                                                                                  \
      if (i < nx0) RX(XN, (x0 + i) & 3, xA, xB, xo);                                                                              \
      if (i >= nx0 && i < nx0 + 8) RW(WN, (i - nx0) & 7, W_, cb);                                                                 \
      if (i >= nx0 + 8 && i < nx0 + nx1 + 8) RX(XN, (x0 + i - 8) & 3, xA, xB, xo); } while (0)
#define R0(i) RDS(i, wa, aW, 0, xh, aA, aB, 0, 0, 2, 2, 3)
#define R1(i) RDS(i, wb, aW, 1, xh, aA, aB, 0, 0, 0, 0, 0)
#define R2(i) RDS(i, wa, bW, 0, xl, bA, bB, 0, 0, 2, 2, 3)
#define R3(i) RDS(i, wb, bW, 1, xc, bA, bB, 4096, 0, 0, 2, 2)
#define R4(i) RDS(i, wa, bW + SB, 1, xc, bA, bB, 4096, 2, 2, 0, 8)
#define R5(i) RDS(i, wc, aW, 1, xh, bA, bB, SB + 4096, 0, 4, 0, 8)
#define R6(i) RDS(i, wb, aW, 0, xl, aA, aB, 4096, 0, 4, 0, 8)
#define R7(i) RDS(i, wa, bW, 0, xl, aA, aB, 0, 0, 0, 0, 0)
    PH(0, 0, 1, xl, wb, R0);
    PH(1, 0, 0, xh, wa, R1);
    BAR();
    SCHED();
    PH(2, 0, 1, xh, wb, R2);
    PH(3, 0, 0, xl, wa, R3);
    PH(4, 0, 1, xl, wb, R4);
    PH(5, 2, 1, xc, wb, R5);
    PH(6, 2, 1, xh, wa, R6);
    PH(7, 2, 1, xl, wc, R7);
#define PL(m) do { _Pragma("unroll") for (int i = 0; i < 16; i++) {                                                               \
      const int e = (m - 2) * 2 + (i & 1), f = ((i >> 1) & 6) + (i & 1);                                                          \
      if (i & 2) MF(m, i >> 2, xc[e], wa[f]); else MF(m, i >> 2, xl[e], wb[f]);                                                   \
      SCHED();                                                                                                                    \
      OUTW(96 + m * 16 + i, (void)0); } } while (0)
    PL(2);
    PL(3);
#pragma unroll
    for (int j = 28; j < 32; j++) { PK(j, 1); SCHED(); OUT_ST(j); SCHED(); }
  } else {
  if (wid < 6) {
    const int h = wid - 4;
    unsigned xv[16];
#pragma unroll
    for (int i = 0; i < 16; i++) xv[i] = (unsigned)((8 * i + (lane >> 3)) * (K_ * 2) + (((lane & 7) ^ ((i & 1) * 4 + (lane >> 4))) << 4));
    const char* xbase = (const char*)(x + (size_t)(tm * BM + h * 128) * K_);
    const unsigned ldsx = (unsigned)(size_t)(shm) + h * 16384;
#define X8(o, l_, b_)                                                                                                             \
      asm volatile("s_mov_b32 m0, %8\n\ts_nop 0\n\tglobal_load_lds_dwordx4 %0, %9\n\t"                                            \
                   "s_add_u32 m0, m0, 0x400\n\ts_nop 0\n\tglobal_load_lds_dwordx4 %1, %9\n\t"                                     \
                   "s_add_u32 m0, m0, 0x400\n\ts_nop 0\n\tglobal_load_lds_dwordx4 %2, %9\n\t"                                     \
                   "s_add_u32 m0, m0, 0x400\n\ts_nop 0\n\tglobal_load_lds_dwordx4 %3, %9\n\t"                                     \
                   "s_add_u32 m0, m0, 0x400\n\ts_nop 0\n\tglobal_load_lds_dwordx4 %4, %9\n\t"                                     \
                   "s_add_u32 m0, m0, 0x400\n\ts_nop 0\n\tglobal_load_lds_dwordx4 %5, %9\n\t"                                     \
                   "s_add_u32 m0, m0, 0x400\n\ts_nop 0\n\tglobal_load_lds_dwordx4 %6, %9\n\t"                                     \
                   "s_add_u32 m0, m0, 0x400\n\ts_nop 0\n\tglobal_load_lds_dwordx4 %7, %9"                                         \
                   :: "v"(xv[o]), "v"(xv[o + 1]), "v"(xv[o + 2]), "v"(xv[o + 3]), "v"(xv[o + 4]), "v"(xv[o + 5]), "v"(xv[o + 6]), "v"(xv[o + 7]), \
                      "s"(l_), "s"(b_) : "memory", "scc")
#define ISSUE_X(s, slot) do { const char* b_ = xbase + kstep_of(s, tm, tn) * 128; const unsigned l_ = ldsx + (slot) * SB;       \
      X8(0, l_, b_); X8(8, l_ + 8192, b_); } while (0)
    BAR();
    BAR();
    BAR();
    ISSUE_X(0, 0);
    ISSUE_X(1, 1);
    WAIT_V(16);
    asm volatile("" :: "v"(ct));
    BAR();
    int xslot = 2;
#pragma unroll 1
    for (int t = -1; t < NT - 2; t++) {
      if (t + 3 < NT) { ISSUE_X(t + 3, xslot); xslot = xslot == NS - 1 ? 0 : xslot + 1; WAIT_V(16); } else { WAIT_V(0); }
      BAR();
    }
  } else {
    QRegs qa, qb; u32x2 sb; unsigned tagv;
    constexpr unsigned RO = 16 * N_ * 4;
    Q_SC(sb, rot + 8); TAG_LOAD(tagv);
    const int io = (lane >> 4) * 1024 + (lane & 15) * 16;
    BAR();
    Q_ROWS(qa, Q_SO(rot + 8));
    Q_ROWS(qb, Q_SO(rot + 8) + RO);
    BAR();
    BAR();
    WAIT_V(8);
    asm volatile("" :: "v"(ct));
    SCALES(sb);
    ROUND(qa, STG_OFF + kc0 * KCB, 0, 0);
    WAIT_V(0);
    const unsigned fvo = (unsigned)(lane * 4);
    unsigned fv = tagv;
    ROUND(qb, STG_OFF + (kc0 + 2) * KCB, 0, 0);
    WAIT_L(0);
    asm volatile("" : "+v"(tagv));
    const unsigned tag = TAG_OF(tagv);
   if (wid == 6) {
    __builtin_amdgcn_s_setprio(3);
    const char* wbase = ws + (size_t)tn * NT * WB;
    const unsigned wv = (unsigned)(lane * 16), wv2 = wv + 4096, wv3 = wv + 8192, wv4 = wv + 12288;
    const unsigned ldsw = (unsigned)(size_t)(shm) + XB;
#define ISSUE_W(s, slot) do { const char* b_ = wbase + (size_t)kstep_of(s, tm, tn) * WB; const unsigned l_ = ldsw + (slot) * SB; \
      asm volatile("s_mov_b32 m0, %4\n\ts_nop 0\n\t"                                                                              \
                   "global_load_lds_dwordx4 %0, %5 sc1\n\tglobal_load_lds_dwordx4 %0, %5 offset:1024 sc1\n\t"                     \
                   "global_load_lds_dwordx4 %0, %5 offset:2048 sc1\n\tglobal_load_lds_dwordx4 %0, %5 offset:3072 sc1\n\t"         \
                   "s_add_u32 m0, m0, 0x1000\n\ts_nop 0\n\t"                                                                      \
                   "global_load_lds_dwordx4 %1, %5 sc1\n\tglobal_load_lds_dwordx4 %1, %5 offset:1024 sc1\n\t"                     \
                   "global_load_lds_dwordx4 %1, %5 offset:2048 sc1\n\tglobal_load_lds_dwordx4 %1, %5 offset:3072 sc1\n\t"         \
                   "s_add_u32 m0, m0, 0x1000\n\ts_nop 0\n\t"                                                                      \
                   "global_load_lds_dwordx4 %2, %5 sc1\n\tglobal_load_lds_dwordx4 %2, %5 offset:1024 sc1\n\t"                     \
                   "global_load_lds_dwordx4 %2, %5 offset:2048 sc1\n\tglobal_load_lds_dwordx4 %2, %5 offset:3072 sc1\n\t"         \
                   "s_add_u32 m0, m0, 0x1000\n\ts_nop 0\n\t"                                                                      \
                   "global_load_lds_dwordx4 %3, %5 sc1\n\tglobal_load_lds_dwordx4 %3, %5 offset:1024 sc1\n\t"                     \
                   "global_load_lds_dwordx4 %3, %5 offset:2048 sc1\n\tglobal_load_lds_dwordx4 %3, %5 offset:3072 sc1"             \
                   :: "v"(wv), "v"(wv2), "v"(wv3), "v"(wv4), "s"(l_), "s"(b_) : "memory", "scc"); } while (0)
#define SET_RDY(f_) do { const unsigned long long b_ = __ballot((f_) == tag);                                                     \
      rda = (unsigned)__builtin_amdgcn_readfirstlane((unsigned)b_); rdb = (unsigned)__builtin_amdgcn_readfirstlane((unsigned)(b_ >> 32)); } while (0)
#define POLL_BLOCK() do { unsigned f_; asm volatile("global_load_dword %0, %1, %2 sc1\n\ts_waitcnt vmcnt(0)" : "=&v"(f_) : "v"(fvo), "s"(flags) : "memory"); \
      SET_RDY(f_); } while (0)
#define READY(s) (((((s) > 8 ? rdb : rda) >> ((kstep_of(s, tm, tn) & 7) * 4)) & 15u) == 15u)
#define NEXT_W() do { nw++; wslot = wslot == NS - 1 ? 0 : wslot + 1; } while (0)
    unsigned rda = 0, rdb = 0, spins = 0;
    BAR();
    int nw = 2, wslot = 2;
    bool ppoll = false, preg = false;
#pragma unroll 1
    for (int t = -1; t < NT - 2; t++) {
      const int lim = t + 3 < NT ? t + 3 : NT - 1;
      if (preg) { WAIT_V(16); } else { WAIT_V(0); }
      asm volatile("" : "+v"(fv));
      if (ppoll) SET_RDY(fv);
      if (nw <= t + 2) {
#pragma unroll 1
        while (!READY(nw)) { if (++spins > SPIN_MAX) { rda = ~0u; rdb = ~0u; break; } POLL_BLOCK(); }
        ISSUE_W(nw, wslot); NEXT_W();
      }
      ppoll = spins <= SPIN_MAX && (t < POLLB_T ? rda != ~0u : rdb != ~0u);
      asm volatile("global_load_dword %0, %1, %2 sc1" : "+v"(fv) : "v"(fvo), "s"(ppoll ? (const char*)flags : (const char*)am) : "memory");
      preg = nw <= lim && READY(nw);
      if (preg) { ISSUE_W(nw, wslot); NEXT_W(); WAIT_V(17); } else { WAIT_V(1); }
      BAR();
    }
    WAIT_V(0);
    asm volatile("" : "+v"(fv));
    if (tm == 0) {
#pragma unroll 1
      while ((rda & rdb) != ~0u && ++spins < SPIN_MAX) POLL_BLOCK();
      if (lane == 0) __hip_atomic_store(flags + 64, tag, RLX_AGENT);
    }
   } else {
    BAR();
    u32x4 bv[16];
#pragma unroll
    for (int i = 0; i < 16; i++) bv[i] = *(const u32x4*)(shm + STG_OFF + lane * 16 + i * 1024);
    WAIT_L(0);
    SCHED();
    BAR();
    const unsigned g_ = (unsigned)((tn * NT + rot + 8) * WB + lane * 16);
#pragma unroll
    for (int i = 0; i < 8; i++) __builtin_amdgcn_raw_buffer_store_b128(bv[i], rsW, g_ + i * 1024, 0, AUX_SC1);
    SCHED();
    BAR();
#pragma unroll
    for (int i = 8; i < 16; i++) __builtin_amdgcn_raw_buffer_store_b128(bv[i], rsW, g_ + i * 1024, 0, AUX_SC1);
    SCHED();
    BAR();
    BAR();
    WAIT_V(0);
    asm volatile("" :: "v"(fv));
    if (lane < 4) __hip_atomic_store(flags + (rot + 8) * 4 + lane, tag, RLX_AGENT);
#pragma unroll 1
    for (int t = 4; t < NT - 1; t++) { BAR(); }
   }
  }
  }
  asm volatile("s_nop 0" ::: "memory");
}

extern "C" void kernel_launch(void* const* d_in, const int* in_sizes, int n_in, void* d_out, int out_size, void* d_ws,
                              size_t ws_size, hipStream_t stream) {
  constexpr int grid = 256;
  nf4_gemm<<<grid, 512, 0, stream>>>((const uint16_t*)d_in[0], (const int*)d_in[1], (const uint16_t*)d_in[2],
                                    (const uint16_t*)d_in[3], (char*)d_ws, (uint16_t*)d_out);
}
```

```cpp
#include <hip/hip_runtime.h>
#include <hip/hip_bf16.h>
#include <stdint.h>

typedef __attribute__((ext_vector_type(8))) short bf16x8;
typedef __attribute__((ext_vector_type(4))) float f32x4;
typedef __attribute__((ext_vector_type(4))) int i32x4;
typedef __attribute__((ext_vector_type(4))) unsigned u32x4;
typedef __attribute__((ext_vector_type(2))) unsigned u32x2;
typedef __attribute__((ext_vector_type(2))) unsigned long long u64x2;
typedef __attribute__((ext_vector_type(2))) float f32x2;
typedef __attribute__((ext_vector_type(2))) __bf16 bf16x2_t;
typedef __attribute__((address_space(1))) unsigned gu32;

constexpr int M_ = 2048, K_ = 1024, N_ = 3584;
constexpr int BM = 256, BN = 128, BK = 64, NT = K_ / BK;
constexpr int XB = BM * BK * 2;
constexpr int WB = BN * BK * 2;
constexpr int KCB = BN * 16;
constexpr int SB = XB + WB;
constexpr int NS = 3;
constexpr int HDR = 2048;
constexpr int LUT_OFF = -HDR;
constexpr int STG_OFF = SB + XB;
constexpr int SHM_BYTES = HDR + NS * SB;
constexpr size_t WIMG_BYTES = (size_t)28 * NT * WB;
constexpr size_t SYNC_OFF = (size_t)8 << 20;
constexpr unsigned SPIN_MAX = 1u << 17;
constexpr int POLLB_T = 5;
constexpr int BI = 12;
#define RPLAN "r.r.rr.r.rr.r.rr.rrc" "r.D.r.I.r.r.r.rpr.r.r.r.......j."
#define TPLAN "................" "................" "......1....2t2.t" "1.s11.s11s2.t1.t" "1.s11.s11s11t1.t" "1.s11.s11t1.t1s2" "t1t1t1t1t1t2t..." "....2t1s.s.t21.."
constexpr int FG = 512;
constexpr unsigned CODE_TOUCH_MAX = 10112;
constexpr int AUX_NT = 2, AUX_SC1 = 16;

#define WAIT_V(n) asm volatile("s_waitcnt vmcnt(%0)" ::"n"(n) : "memory")
#define WAIT_L(n) asm volatile("s_waitcnt lgkmcnt(%0)" ::"n"(n) : "memory")
#define WAIT_LB(n) __builtin_amdgcn_s_waitcnt(0xC07F | ((n) << 8))
#define BAR() do { asm volatile("" ::: "memory"); __builtin_amdgcn_s_barrier(); asm volatile("" ::: "memory"); } while (0)
#define SCHED() __builtin_amdgcn_sched_barrier(0)
#define TAG_OF(e) ((e) * 0x9E3779B1u + 0x7F4A7C15u)
#define RLX_AGENT __ATOMIC_RELAXED, __HIP_MEMORY_SCOPE_AGENT

__device__ __forceinline__ unsigned pack_bf16(float a, float b) {
  f32x2 v = {a, b};
  bf16x2_t r = __builtin_convertvector(v, bf16x2_t);
  return __builtin_bit_cast(unsigned, r);
}
__device__ __forceinline__ int kstep_of(int s, int tm) {
  const int u = (int)(0xFEDCBA9765432180ull >> (4 * s)) & 15;
  return (u & 8) + ((tm + u) & 7);
}

__device__ __forceinline__ void pin_acc(f32x4& a, int k) {
  switch (k) {
    case 0: asm volatile("" : "+{v[2:5]}"(a)); break;
    case 1: asm volatile("" : "+{v[6:9]}"(a)); break;
    case 2: asm volatile("" : "+{v[10:13]}"(a)); break;
    case 3: asm volatile("" : "+{v[14:17]}"(a)); break;
    case 4: asm volatile("" : "+{v[18:21]}"(a)); break;
    case 5: asm volatile("" : "+{v[22:25]}"(a)); break;
    case 6: asm volatile("" : "+{v[26:29]}"(a)); break;
    case 7: asm volatile("" : "+{v[30:33]}"(a)); break;
    case 8: asm volatile("" : "+{v[34:37]}"(a)); break;
    case 9: asm volatile("" : "+{v[38:41]}"(a)); break;
    case 10: asm volatile("" : "+{v[42:45]}"(a)); break;
    case 11: asm volatile("" : "+{v[46:49]}"(a)); break;
    case 12: asm volatile("" : "+{v[50:53]}"(a)); break;
    case 13: asm volatile("" : "+{v[54:57]}"(a)); break;
    case 14: asm volatile("" : "+{v[58:61]}"(a)); break;
    case 15: asm volatile("" : "+{v[62:65]}"(a)); break;
    case 16: asm volatile("" : "+{v[66:69]}"(a)); break;
    case 17: asm volatile("" : "+{v[70:73]}"(a)); break;
    case 18: asm volatile("" : "+{v[74:77]}"(a)); break;
    case 19: asm volatile("" : "+{v[78:81]}"(a)); break;
    case 20: asm volatile("" : "+{v[82:85]}"(a)); break;
    case 21: asm volatile("" : "+{v[86:89]}"(a)); break;
    case 22: asm volatile("" : "+{v[90:93]}"(a)); break;
    case 23: asm volatile("" : "+{v[94:97]}"(a)); break;
    case 24: asm volatile("" : "+{v[98:101]}"(a)); break;
    case 25: asm volatile("" : "+{v[102:105]}"(a)); break;
    case 26: asm volatile("" : "+{v[106:109]}"(a)); break;
    case 27: asm volatile("" : "+{v[110:113]}"(a)); break;
    case 28: asm volatile("" : "+{v[114:117]}"(a)); break;
    case 29: asm volatile("" : "+{v[118:121]}"(a)); break;
    case 30: asm volatile("" : "+{v[122:125]}"(a)); break;
    case 31: asm volatile("" : "+{v[126:129]}"(a)); break;
  }
}
__device__ __forceinline__ void wait_lb(int n) {
  switch (n) {
    case 0: WAIT_LB(0); break;   case 1: WAIT_LB(1); break;   case 2: WAIT_LB(2); break;   case 3: WAIT_LB(3); break;
    case 4: WAIT_LB(4); break;   case 5: WAIT_LB(5); break;   case 6: WAIT_LB(6); break;   case 7: WAIT_LB(7); break;
    case 8: WAIT_LB(8); break;   case 9: WAIT_LB(9); break;   case 10: WAIT_LB(10); break; case 11: WAIT_LB(11); break;
    case 12: WAIT_LB(12); break; case 13: WAIT_LB(13); break; case 14: WAIT_LB(14); break;
  }
}
struct TailPlan {
  int st[128], pk[129], pj[64], pd[64];
  constexpr TailPlan() : st(), pk(), pj(), pd() {
    for (int k = 0; k < 64; k++) { pj[k] = k < 48 ? k >> 1 : 24 + ((k - 48) >> 3) * 4 + (k & 3); pd[k] = k < 48 ? k & 1 : (k >> 2) & 1; }
    const char* const plan =
        TPLAN;
    int k = 0, j = 0;
    for (int u = 0; u < 128; u++) {
      const char c = plan[u];
      pk[u] = k; st[u] = (c == 's' || c == 't') ? j++ : -1;
      k += c == '2' ? 2 : (c == '1' || c == 't') ? 1 : 0;
    }
    pk[128] = k;
  }
};
constexpr TailPlan tl;
static_assert(tl.pk[128] == 60, "");
struct ReadPlan {
  int rd[64], wt[64], nf[32], n2;
  constexpr ReadPlan() : rd(), wt(), nf(), n2(0) {
    const char* const plan = RPLAN;
    int k = 0;
    for (int u = 0; u < 64 - BI; u++) {
      const char c = plan[u];
      if (u == 32 - BI) n2 = k;
      if (u >= 32 - BI) nf[u - (32 - BI)] = k - n2;
      wt[u] = -1; rd[u] = -1;
      if (c != 'r' && c != '.') {
        const int i = (u - (32 - BI) + ((c | 32) - 'a' + 1)) | 1;
        const int need = i < 1 ? 2 : i < 10 ? i + 3 : i < 16 ? 12 : i < 17 ? 13 : i < 24 ? 14 : i < 25 ? 15 : i < 32 ? 16 : i < 40 ? i - 15 : 24;
        wt[u] = k - need;
      }
      if (c == 'r' || (c >= 'A' && c <= 'Z')) rd[u] = k++;
    }
    for (int u = 64 - BI; u < 64; u++) { rd[u] = -1; wt[u] = -1; }
    if (k != 24) n2 = -1;
  }
};
constexpr ReadPlan rp;
static_assert(rp.n2 >= 12 && rp.wt[31 - BI] >= 0, "");
struct QRegs { i32x4 r[8]; };
struct LV { f32x2 v[4][4]; float sc[4]; };
__device__ __forceinline__ f32x2 pair_lookup(const char* lut, int q0, int q1) { return *(const f32x2*)(lut + (q0 << 3) + (q1 << 7)); }
__device__ __forceinline__ u32x4 q_pack(const LV& l, int i) {
  u32x4 o;
  o.x = pack_bf16(l.v[i][0].x * l.sc[i], l.v[i][0].y * l.sc[i]); o.y = pack_bf16(l.v[i][1].x * l.sc[i], l.v[i][1].y * l.sc[i]);
  o.z = pack_bf16(l.v[i][2].x * l.sc[i], l.v[i][2].y * l.sc[i]); o.w = pack_bf16(l.v[i][3].x * l.sc[i], l.v[i][3].y * l.sc[i]);
  return o;
}

__global__ __launch_bounds__(512, 2) void nf4_gemm(const uint16_t* x_, const int* qw_, const uint16_t* cb_, const uint16_t* am_,
                                                   char* ws_, uint16_t* out_) {
  __shared__ __attribute__((aligned(1024))) char shm_raw[SHM_BYTES];
  char* const shm = shm_raw + HDR;
  u32x4 rsQ; u64x2 k2, k4; unsigned long long k0;
  asm volatile("s_load_dwordx2 s[20:21], %4, 8\n\ts_load_dwordx4 %1, %4, 16\n\ts_load_dwordx4 %2, %4, 32\n\ts_load_dwordx2 %3, %4, 0\n\t"
               "s_mov_b32 s22, %5\n\ts_mov_b32 s23, 0x20000"
               : "=&{s[20:23]}"(rsQ), "=&{s[8:11]}"(k2), "=&{s[16:19]}"(k4), "=&{s[4:5]}"(k0)
               : "s"(__builtin_amdgcn_kernarg_segment_ptr()), "n"(K_ * N_ * 4));
#define ARGS() asm volatile("s_waitcnt lgkmcnt(0)" : "+{s[20:23]}"(rsQ), "+{s[8:11]}"(k2), "+{s[16:19]}"(k4), "+{s[4:5]}"(k0))
#define PTRS() asm volatile("" : "+s"(k2), "+s"(k4), "+s"(k0));                                                                    \
  const char* const x = (const char*)k0; const char* const am = (const char*)k2.y; char* const ws = (char*)k4.x;                  \
  gu32* const flags = (gu32*)(ws + SYNC_OFF + (size_t)tn * FG);                                                                   \
  const __amdgpu_buffer_rsrc_t rsW = __builtin_amdgcn_make_buffer_rsrc((void*)ws, (short)0, (int)WIMG_BYTES, 0x00020000);         \
  (void)x; (void)am; (void)flags; (void)rsW
  asm volatile("s_cmp_lt_u32 %0, 32\n\ts_cbranch_scc1 .Lnf4_prefetch" :: "{s2}"(blockIdx.x), "{v0}"(threadIdx.x), "{s[4:5]}"(k0) : "scc");
  unsigned long long pc0; asm volatile("s_getpc_b64 %0" : "=s"(pc0));
  unsigned ct;
  asm volatile("global_load_dword %0, %1, %2" : "=&v"(ct) : "v"(min((unsigned)(threadIdx.x * 128u), CODE_TOUCH_MAX)), "s"(pc0) : "memory");
  const int tm = (blockIdx.x & 3) * 2 + ((blockIdx.x >> 3) & 1), tn = (blockIdx.x >> 4) + (blockIdx.x & 4 ? 12 : -2);
  const int rot = tm;
  const int tid = threadIdx.x, lane = tid & 63;
  const int wid = __builtin_amdgcn_readfirstlane(tid >> 6);
  const int nc = lane & 31;
  const int kc0 = wid < 4 ? 2 * wid : 4 * (wid & 1);
  const int myks = wid < 4 ? rot : rot + 8;
  unsigned qvoff = (unsigned)(((kc0 + (lane >> 5)) * 8 * N_ + nc * 4) * 4);
  unsigned avoff = (unsigned)((myks * N_ + tn * BN) * 2 + nc * 8);
  unsigned evoff = (unsigned)(SYNC_OFF + tn * FG + 256);
  unsigned qso = (unsigned)(tn * BN * 4 + myks * (BK * N_ * 4));
  unsigned co0 = (unsigned)((lane & 15) * 2), co1 = (unsigned)((tid >> 4) * 2);
  asm volatile("" : "+v"(qvoff), "+v"(avoff), "+v"(evoff), "+s"(qso), "+v"(co0), "+v"(co1));
  SCHED();
  ARGS();
  char* lut = shm + LUT_OFF;
#define Q_ROW(Q, j, so_) asm volatile("buffer_load_dwordx4 %0, %1, %2, %3 offen nt" : "=&v"(Q.r[j]) : "v"(qvoff), "s"(rsQ), "s"((so_) + (j) * (N_ * 4)) : "memory")
#define Q_ROWS(Q, so_) do { _Pragma("unroll") for (int j_ = 0; j_ < 8; j_++) Q_ROW(Q, j_, so_); } while (0)
#define Q_SC(S) asm volatile("global_load_dwordx2 %0, %1, %2" : "=&v"(S) : "v"(avoff), "s"(k2.y) : "memory")
#define TAG_LOAD(T) asm volatile("global_load_dword %0, %1, %2 sc1" : "=&v"(T) : "v"(evoff), "s"(k4.x) : "memory")
#define Q_PIN(q) asm volatile("" : "+v"(q.r[0]), "+v"(q.r[1]), "+v"(q.r[2]), "+v"(q.r[3]), "+v"(q.r[4]), "+v"(q.r[5]), "+v"(q.r[6]), "+v"(q.r[7]))
#define SCALES(S) do { asm volatile("" : "+v"(S));                                                                                \
      lv.sc[0] = __builtin_bit_cast(float, S.x << 16); lv.sc[1] = __builtin_bit_cast(float, S.x & 0xffff0000u);                   \
      lv.sc[2] = __builtin_bit_cast(float, S.y << 16); lv.sc[3] = __builtin_bit_cast(float, S.y & 0xffff0000u); } while (0)
#define ROUND(Q, dst, PUB, g_) do { Q_PIN(Q);                                                                                     \
      _Pragma("unroll") for (int i_ = 0; i_ < 4; i_++) _Pragma("unroll") for (int a_ = 0; a_ < 4; a_++)                           \
        lv.v[i_][a_] = pair_lookup(lut, Q.r[2 * a_][i_], Q.r[2 * a_ + 1][i_]);                                                    \
      SCHED();                                                                                                                    \
      char* d_ = shm + (dst) + io;                                                                                                \
      _Pragma("unroll") for (int i_ = 0; i_ < 4; i_++) { const u32x4 o_ = q_pack(lv, i_);                                         \
        if (PUB) { __builtin_amdgcn_raw_buffer_store_b128(o_, rsW, (g_) + i_ * 256, 0, AUX_SC1); SCHED(); }                       \
        *(u32x4*)(d_ + i_ * 256) = o_; }                                                                                          \
      SCHED(); } while (0)
  LV lv;

  if (__builtin_expect(wid < 4, 1)) {
    QRegs q; u32x2 qsv; unsigned c0, c1, tagv;
    asm volatile("global_load_ushort %0, %2, %4\n\tglobal_load_ushort %1, %3, %4" : "=&v"(c0), "=&v"(c1) : "v"(co0), "v"(co1), "s"(k2.x) : "memory");
    Q_ROWS(q, qso);
    Q_SC(qsv);
    TAG_LOAD(tagv);
    SCHED();
    PTRS();
    BAR();
    const int io = (lane >> 4) * 1024 + (lane & 15) * 16;
    unsigned gA = (unsigned)((tn * NT + rot) * WB + kc0 * KCB + io);
    asm volatile("" : "+v"(gA));
    f32x4 acc[4][8];
#pragma unroll
    for (int m = 0; m < 4; m++)
#pragma unroll
      for (int n = 0; n < 8; n++) { acc[m][n] = (f32x4){0.f, 0.f, 0.f, 0.f}; pin_acc(acc[m][n], m * 8 + n); }
    WAIT_V(10);
    asm volatile("" : "+v"(c0), "+v"(c1)); asm volatile("" :: "v"(ct));
    *(u32x2*)(lut + tid * 8) = (u32x2){c0 << 16, c1 << 16};
    WAIT_L(0);
    BAR();
    WAIT_V(0);
    SCALES(qsv);
    ROUND(q, XB + kc0 * KCB, 1, gA);
    BAR();
    SCHED();
    const int fr = lane & 15, fq = lane >> 4;
    const int xo0 = (wid * 64 + fr) * 128 + ((fq ^ (fr >> 1)) << 4), xo1 = xo0 ^ 64;
    const int wo = XB + fq * KCB + (fr << 4);
    bf16x8 xl[4], xh[4], wa[8], wb[8];
    typedef __attribute__((address_space(3))) const bf16x8 lds_frag;
#define LD(a_) (*(lds_frag*)(a_))
#define MF(m_, n_, X, W) do { acc[m_][n_] = __builtin_amdgcn_mfma_f32_16x16x32_bf16(X, W, acc[m_][n_], 0, 0, 0); pin_acc(acc[m_][n_], (m_) * 8 + (n_)); } while (0)
#define RW(wf, j_, W_, cb) wf[j_] = LD((W_) + ((j_) & 1) * (4 * KCB) + ((cb) * 4 + ((j_) >> 1)) * 256)
#define RX(xf, j_, A, B, off) xf[j_] = LD((((j_) & 1) ? (B) : (A)) + (off) + ((j_) >> 1) * 2048)
#define KH(i) (((i) ^ ((i) >> 1)) & 1)
#define MP(m0, cb, i, X, W) MF((m0) + (((i) >> 3) & 1), (cb) * 4 + (((i) >> 1) & 3), X[(((i) >> 2) & 2) + KH(i)], W[((i) & 6) + KH(i)])
#define VADD(d, v_, s_) asm volatile("v_add_u32 %0, %1, %2" : "=v"(d) : "s"(s_), "v"(v_))
    const unsigned lb = (unsigned)(size_t)shm;
    unsigned bW = lb + wo, bA = lb + xo0, bB = lb + xo1;
    unsigned vf = (unsigned)(((tm * BM + wid * 64 + 4 * fq) * N_ + tn * BN) * 2 + fr * 8);
    asm volatile("" : "+v"(bW), "+v"(bA), "+v"(bB), "+v"(vf));
    __amdgpu_buffer_rsrc_t rsO = __builtin_amdgcn_make_buffer_rsrc((void*)k4.y, (short)0, M_ * N_ * 2, 0x00020000);
    asm volatile("" : "+s"(rsO));
    unsigned ro[16];
#pragma unroll
    for (int j = 0; j < 16; j++) { ro[j] = (unsigned)(((j >> 2) * 16 + (j & 3)) * (N_ * 2)); asm volatile("" : "+s"(ro[j])); }
    WAIT_V(0);
    asm volatile("" : "+v"(tagv));
    if (lane == 0) __hip_atomic_store(flags + (rot * 4 + wid), TAG_OF(tagv), RLX_AGENT);
    unsigned aW = bW, aA = bA, aB = bB;
    asm volatile("" : "+v"(aW), "+v"(aA), "+v"(aB));
    unsigned so_ = 0;
    WAIT_L(0);
    SCHED();
    BAR();
#define RK(k) do { if (k == 0 || k == 2) RX(xl, (k >> 1) & 1, aA, aB, 0); else if (k == 1) RW(wa, 0, aW, 0);                       \
      else if (k < 10) RW(wa, (k - 2) & 7, aW, 0); else if (k < 12) RX(xl, (k - 8) & 3, aA, aB, 0);                               \
      else if (k < 16) RX(xh, k & 3, aA, aB, 4096); else RW(wb, k & 7, aW, 1); } while (0)
#pragma unroll
    for (int k = 0; k < rp.n2; k++) { RK(k); SCHED(); }
    asm volatile(".p2align 5\n\t.rept %0\n\ts_nop 0\n\t.endr" :: "n"(6));
    SCHED();
    WAIT_LB(rp.wt[31 - BI]);
    SCHED();
#pragma unroll 1
    for (int t = 0; t < NT - 2;) {
#pragma unroll
      for (int i = 0; i < 32; i++) {
        if (i < 16) MP(0, 0, i, xl, wa); else MP(2, 0, i, xh, wa);
        SCHED();
        wait_lb(rp.wt[32 - BI + i]);
        if (rp.rd[32 - BI + i] >= 0) RK(rp.rd[32 - BI + i]);
        SCHED();
      }
#pragma unroll
      for (int i = 0; i < 32; i++) {
        if (i == 31) { WAIT_LB(rp.wt[31 - BI]); SCHED(); }
        if (i < 16) MP(0, 1, i, xl, wb); else MP(2, 1, i, xh, wb);
        SCHED();
        if (i == 0) { so_ = so_ == 2 * SB ? 0 : so_ + SB; asm volatile("" : "+s"(so_)); }
        if (i == 3) VADD(aW, bW, so_);
        if (i == 5) VADD(aA, bA, so_);
        if (i == 7) VADD(aB, bB, so_);
        if (i == 4) { t++; asm volatile("" : "+s"(t)); }
        if (i == BI - 1) BAR();
        if (i >= BI && rp.rd[(i - BI) & 63] >= 0) RK(rp.rd[(i - BI) & 63]);
        SCHED();
      }
    }
    u32x2 ev[32];
    bf16x8 wc[8], xc[4];
#define PC_M(j) (((j) >> 4) * 2 + (((j) >> 2) & 1))
#define PC_C(j) ((((j) >> 3) ^ ((j) >> 4)) & 1)
#define PK(j, d) ev[j][d] = pack_bf16(acc[PC_M(j)][PC_C(j) * 4 + 2 * (d)][(j) & 3], acc[PC_M(j)][PC_C(j) * 4 + 2 * (d) + 1][(j) & 3])
#define OUT_ST(j) __builtin_amdgcn_raw_buffer_store_b64(ev[j], rsO, vf + PC_C(j) * 128, ro[PC_M(j) * 4 + ((j) & 3)], AUX_NT)
#define PH(p, m0, cb, XC, WC, RD) do { _Pragma("unroll") for (int i = 0; i < 16; i++) {                                           \
      MP(m0, cb, i, XC, WC); SCHED();                                                                                             \
      OUTW(p * 16 + i, RD(i)); } } while (0)
#define OUTW(u, R_) do {                                                                                                          \
      if (tl.st[u] >= 0) { OUT_ST(tl.st[u] & 31); SCHED(); }                                                                      \
      R_; SCHED();                                                                                                                \
      if (tl.pk[(u) + 1] > tl.pk[u]) PK(tl.pj[tl.pk[u]], tl.pd[tl.pk[u]]);                                                        \
      if (tl.pk[(u) + 1] > tl.pk[u] + 1) PK(tl.pj[tl.pk[u] + 1], tl.pd[tl.pk[u] + 1]);                                            \
      SCHED();                                                                                                                    \
      if (tl.st[u] >= 0) asm volatile("" :: "v"(ev[tl.st[u] & 31])); } while (0)
#define RDS(i, WN, W_, cb, XN, xA, xB, xo, x0, nx0, nx1, w14) do {                                                                \
      if (i == 5) WAIT_LB(5);                                                                                                     \
      if (i == 14) WAIT_LB(w14);                                                                                                  \
      if (i < nx0) RX(XN, (x0 + i) & 3, xA, xB, xo);                                                                              \
      if (i >= nx0 && i < nx0 + 8) RW(WN, (i - nx0) & 7, W_, cb);                                                                 \
      if (i >= nx0 + 8 && i < nx0 + nx1 + 8) RX(XN, (x0 + i - 8) & 3, xA, xB, xo); } while (0)
#define R0(i) do { if (i < 5 && rp.wt[32 - BI + i] >= 0) wait_lb(rp.wt[32 - BI + i] - rp.nf[i] + i);                              \
      RDS(i, wb, aW, 1, xh, aA, aB, 0, 0, 0, 0, 0); } while (0)
#define R1(i) RDS(i, wa, bW, 0, xh, bA, bB, 0, 0, 2, 2, 3)
#define R2(i) RDS(i, wb, bW, 1, xc, bA, bB, 4096, 0, 0, 2, 2)
#define R3(i) RDS(i, wc, aW, 1, xc, bA, bB, 4096, 2, 2, 0, 8)
#define R4(i) RDS(i, wa, aW, 0, xl, aA, aB, 4096, 0, 4, 0, 8)
#define R5(i) RDS(i, wb, bW, 0, xl, aA, aB, 0, 0, 0, 0, 0)
    PH(0, 0, 0, xl, wa, R0);
    BAR();
    SCHED();
    PH(1, 0, 1, xl, wb, R1);
    PH(2, 0, 0, xh, wa, R2);
    PH(3, 0, 1, xh, wb, R3);
    PH(4, 2, 1, xc, wb, R4);
    PH(5, 2, 1, xl, wc, R5);
#define PL(m) do { _Pragma("unroll") for (int i = 0; i < 16; i++) {                                                               \
      const int e = (m - 2) * 2 + (i & 1), f = ((i >> 1) & 6) + (i & 1);                                                          \
      if (i & 2) MF(m, i >> 2, xc[e], wb[f]); else MF(m, i >> 2, xl[e], wa[f]);                                                   \
      SCHED();                                                                                                                    \
      OUTW(64 + m * 16 + i, (void)0); } } while (0)
    PL(2);
    PL(3);
#pragma unroll
    for (int j = 28; j < 32; j++) { PK(j, 1); SCHED(); OUT_ST(j); SCHED(); }
  } else {
  PTRS();
  if (wid < 6) {
    const int h = wid - 4;
    unsigned xv[16];
#pragma unroll
    for (int i = 0; i < 16; i++) xv[i] = (unsigned)((8 * i + (lane >> 3)) * (K_ * 2) + (((lane & 7) ^ ((i & 1) * 4 + (lane >> 4))) << 4));
    const char* xbase = x + (size_t)(tm * BM + h * 128) * (K_ * 2);
    const unsigned ldsx = (unsigned)(size_t)(shm) + h * 16384;
#define X8(o, l_, b_)                                                                                                             \
      asm volatile("s_mov_b32 m0, %8\n\ts_nop 0\n\tglobal_load_lds_dwordx4 %0, %9\n\t"                                            \
                   "s_add_u32 m0, m0, 0x400\n\ts_nop 0\n\tglobal_load_lds_dwordx4 %1, %9\n\t"                                     \
                   "s_add_u32 m0, m0, 0x400\n\ts_nop 0\n\tglobal_load_lds_dwordx4 %2, %9\n\t"                                     \
                   "s_add_u32 m0, m0, 0x400\n\ts_nop 0\n\tglobal_load_lds_dwordx4 %3, %9\n\t"                                     \
                   "s_add_u32 m0, m0, 0x400\n\ts_nop 0\n\tglobal_load_lds_dwordx4 %4, %9\n\t"                                     \
                   "s_add_u32 m0, m0, 0x400\n\ts_nop 0\n\tglobal_load_lds_dwordx4 %5, %9\n\t"                                     \
                   "s_add_u32 m0, m0, 0x400\n\ts_nop 0\n\tglobal_load_lds_dwordx4 %6, %9\n\t"                                     \
                   "s_add_u32 m0, m0, 0x400\n\ts_nop 0\n\tglobal_load_lds_dwordx4 %7, %9"                                         \
                   :: "v"(xv[o]), "v"(xv[o + 1]), "v"(xv[o + 2]), "v"(xv[o + 3]), "v"(xv[o + 4]), "v"(xv[o + 5]), "v"(xv[o + 6]), "v"(xv[o + 7]), \
                      "s"(l_), "s"(b_) : "memory", "scc")
#define ISSUE_X(s, slot) do { const char* b_ = xbase + kstep_of(s, tm) * 128; const unsigned l_ = ldsx + (slot) * SB;       \
      X8(0, l_, b_); X8(8, l_ + 8192, b_); } while (0)
    BAR();
    BAR();
    BAR();
    ISSUE_X(0, 0);
    ISSUE_X(1, 1);
    WAIT_V(16);
    asm volatile("" :: "v"(ct));
    BAR();
    int xslot = 2;
#pragma unroll 1
    for (int t = -1; t < NT - 2; t++) {
      if (t + 3 < NT) { ISSUE_X(t + 3, xslot); xslot = xslot == NS - 1 ? 0 : xslot + 1; WAIT_V(16); } else { WAIT_V(0); }
      BAR();
    }
  } else {
    QRegs qa, qb; u32x2 sb; unsigned tagv;
    constexpr unsigned RO = 16 * N_ * 4;
    Q_SC(sb); TAG_LOAD(tagv);
    const int io = (lane >> 4) * 1024 + (lane & 15) * 16;
    const int fks = kstep_of(lane >> 2, tm);
    unsigned fvo = (unsigned)(fks * 16 + (fks < 8 ? (lane & 3) * 4 : 0));
    asm volatile("" : "+v"(fvo));
    BAR();
    Q_ROWS(qa, qso);
    Q_ROWS(qb, qso + RO);
    BAR();
    BAR();
    WAIT_V(8);
    asm volatile("" :: "v"(ct));
    SCALES(sb);
    ROUND(qa, STG_OFF + kc0 * KCB, 0, 0);
    WAIT_V(0);
    unsigned fv = tagv;
#define POLL(f_, m_, b_) asm volatile("s_mov_b64 exec, %3\n\tglobal_load_dword %0, %1, %2 sc1\n\ts_mov_b64 exec, -1" : "+v"(f_) : "v"(fvo), "s"(b_), "s"(m_) : "memory")
    ROUND(qb, STG_OFF + (kc0 + 2) * KCB, 0, 0);
    WAIT_L(0);
    asm volatile("" : "+v"(tagv));
    const unsigned tag = TAG_OF(tagv);
   if (wid == 6) {
    __builtin_amdgcn_s_setprio(3);
    const char* wbase = ws + (size_t)tn * NT * WB;
    const unsigned wv = (unsigned)(lane * 16), wv2 = wv + 4096, wv3 = wv + 8192, wv4 = wv + 12288;
    const unsigned ldsw = (unsigned)(size_t)(shm) + XB;
#define ISSUE_W(s, slot) do { const char* b_ = wbase + (size_t)kstep_of(s, tm) * WB; const unsigned l_ = ldsw + (slot) * SB; \
      asm volatile("s_mov_b32 m0, %4\n\ts_nop 0\n\t"                                                                              \
                   "global_load_lds_dwordx4 %0, %5 sc1\n\tglobal_load_lds_dwordx4 %0, %5 offset:1024 sc1\n\t"                     \
                   "global_load_lds_dwordx4 %0, %5 offset:2048 sc1\n\tglobal_load_lds_dwordx4 %0, %5 offset:3072 sc1\n\t"         \
                   "s_add_u32 m0, m0, 0x1000\n\ts_nop 0\n\t"                                                                      \
                   "global_load_lds_dwordx4 %1, %5 sc1\n\tglobal_load_lds_dwordx4 %1, %5 offset:1024 sc1\n\t"                     \
                   "global_load_lds_dwordx4 %1, %5 offset:2048 sc1\n\tglobal_load_lds_dwordx4 %1, %5 offset:3072 sc1\n\t"         \
                   "s_add_u32 m0, m0, 0x1000\n\ts_nop 0\n\t"                                                                      \
                   "global_load_lds_dwordx4 %2, %5 sc1\n\tglobal_load_lds_dwordx4 %2, %5 offset:1024 sc1\n\t"                     \
                   "global_load_lds_dwordx4 %2, %5 offset:2048 sc1\n\tglobal_load_lds_dwordx4 %2, %5 offset:3072 sc1\n\t"         \
                   "s_add_u32 m0, m0, 0x1000\n\ts_nop 0\n\t"                                                                      \
                   "global_load_lds_dwordx4 %3, %5 sc1\n\tglobal_load_lds_dwordx4 %3, %5 offset:1024 sc1\n\t"                     \
                   "global_load_lds_dwordx4 %3, %5 offset:2048 sc1\n\tglobal_load_lds_dwordx4 %3, %5 offset:3072 sc1"             \
                   :: "v"(wv), "v"(wv2), "v"(wv3), "v"(wv4), "s"(l_), "s"(b_) : "memory", "scc"); } while (0)
#define POLL_BLOCK(m_) do { unsigned f_ = ~tag; POLL(f_, m_, flags); asm volatile("s_waitcnt vmcnt(0)" : "+v"(f_) :: "memory"); rdy |= __ballot(f_ == tag); } while (0)
#define READY(s) (((unsigned)(rdy >> (4 * (s))) & 15u) == 15u)
#define NEXT_W() do { nw++; wslot = wslot == NS - 1 ? 0 : wslot + 1; } while (0)
    unsigned long long rdy = 0, want = 0xff00ull;
    unsigned spins = 0;
    BAR();
    int nw = 2, wslot = 2;
    bool preg = false;
#pragma unroll 1
    for (int t = -1; t < NT - 2; t++) {
      const int lim = t + 3 < NT ? t + 3 : NT - 1;
      if (preg) { WAIT_V(16); } else { WAIT_V(0); }
      asm volatile("" : "+v"(fv));
      rdy |= __ballot(fv == tag);
      if (nw <= t + 2) {
#pragma unroll 1
        while (!READY(nw)) { POLL_BLOCK((0xffull << (4 * nw)) & ~rdy); if (++spins > SPIN_MAX) rdy = ~0ull; }
        ISSUE_W(nw, wslot); NEXT_W();
      }
      if (t == 0) want = 0xfffffff00ull;
      if (t == POLLB_T) want = ~0xffull;
      const unsigned long long m = want & ~rdy;
      POLL(fv, m ? m : 1ull, m ? (const char*)flags : (const char*)am);
      preg = nw <= lim && READY(nw);
      if (preg) { ISSUE_W(nw, wslot); NEXT_W(); WAIT_V(17); } else { WAIT_V(1); }
      BAR();
    }
    WAIT_V(0);
    asm volatile("" : "+v"(fv));
    if (tm == 0 && lane == 0) __hip_atomic_store(flags + 64, tag, RLX_AGENT);
   } else {
    BAR();
    u32x4 bv[16];
#pragma unroll
    for (int i = 0; i < 16; i++) bv[i] = *(const u32x4*)(shm + STG_OFF + lane * 16 + i * 1024);
    WAIT_L(0);
    SCHED();
    const unsigned g_ = (unsigned)((tn * NT + rot + 8) * WB + lane * 16);
#pragma unroll
    for (int t = 0; t <= 4; t++) {
      BAR();
      if (t == 1 || t == 2) {
        __builtin_amdgcn_s_sleep(10);
#pragma unroll
        for (int i = (t - 1) * 8; i < t * 8; i++) __builtin_amdgcn_raw_buffer_store_b128(bv[i], rsW, g_ + i * 1024, 0, AUX_SC1);
        SCHED();
      }
    }
    WAIT_V(0);
    asm volatile("" :: "v"(fv));
    if (lane == 0) __hip_atomic_store(flags + (rot + 8) * 4, tag, RLX_AGENT);
#pragma unroll 1
    for (int t = 5; t < NT - 1; t++) BAR();
   }
  }
  }
  asm volatile("s_endpgm\n.Lnf4_prefetch:\n\t"
               "s_lshl_b32 s3, s2, 1\n\ts_and_b32 s3, s3, 6\n\ts_bfe_u32 s0, s2, 0x10003\n\ts_or_b32 s3, s3, s0\n\t"
               "v_lshlrev_b32 v2, 10, v0\n\tv_and_b32 v2, 0xfffff800, v2\n\tv_and_b32 v3, 1, v0\n\tv_lshl_or_b32 v2, v3, 6, v2\n\t"
               "v_lshl_or_b32 v2, s3, 19, v2\n\t"
               "s_mov_b32 s1, 0xfdb96428\n\ts_cmp_lt_u32 s2, 16\n\ts_cmov_b32 s1, 0xeca75310\n\ts_mov_b32 s2, %1\n\ts_waitcnt lgkmcnt(0)\n\ts_sleep 44\n"
               "1:\n\ts_add_u32 s0, s1, s3\n\ts_xor_b32 s0, s0, s1\n\ts_and_b32 s0, s0, 7\n\ts_xor_b32 s0, s0, s1\n\ts_and_b32 s0, s0, 15\n\t"
               "v_lshl_add_u32 v3, s0, 7, v2\n\tglobal_load_dword v4, v3, s[4:5]\n"
               "2:\n\ts_sleep %0\n\ts_sub_u32 s2, s2, 1\n\ts_cbranch_scc0 2b\n\ts_mov_b32 s2, 0\n\t"
               "s_lshr_b32 s1, s1, 4\n\ts_cbranch_scc1 1b\n\ts_waitcnt vmcnt(0)"
               :: "n"(12), "n"(2) : "memory");
}

extern "C" void kernel_launch(void* const* d_in, const int* in_sizes, int n_in, void* d_out, int out_size, void* d_ws,
                              size_t ws_size, hipStream_t stream) {
  nf4_gemm<<<256, 512, 0, stream>>>((const uint16_t*)d_in[0], (const int*)d_in[1], (const uint16_t*)d_in[2],
                                   (const uint16_t*)d_in[3], (char*)d_ws, (uint16_t*)d_out);
}
```

```cpp
#include <hip/hip_runtime.h>
#include <hip/hip_bf16.h>
#include <stdint.h>

typedef __attribute__((ext_vector_type(8))) short bf16x8;
typedef __attribute__((ext_vector_type(4))) float f32x4;
typedef __attribute__((ext_vector_type(4))) int i32x4;
typedef __attribute__((ext_vector_type(4))) unsigned u32x4;
typedef __attribute__((ext_vector_type(2))) unsigned u32x2;
typedef __attribute__((ext_vector_type(2))) unsigned long long u64x2;
typedef __attribute__((ext_vector_type(2))) float f32x2;
typedef __attribute__((ext_vector_type(2))) __bf16 bf16x2_t;
typedef __attribute__((address_space(1))) unsigned gu32;

constexpr int M_ = 2048, K_ = 1024, N_ = 3584;
constexpr int BM = 256, BN = 128, BK = 64, NT = K_ / BK;
constexpr int XB = BM * BK * 2;
constexpr int WB = BN * BK * 2;
constexpr int KCB = BN * 16;
constexpr int SB = XB + WB;
constexpr int NS = 3;
constexpr int HDR = 2048;
constexpr int LUT_OFF = -HDR;
constexpr int STG_OFF = SB + XB;
constexpr int SHM_BYTES = HDR + NS * SB;
constexpr size_t WIMG_BYTES = (size_t)16 << 20;
constexpr size_t SYNC_OFF = WIMG_BYTES;
constexpr unsigned SPIN_MAX = 1u << 17;
constexpr int POLLB_T = 5;
constexpr int BI = 12;
#define RPLAN "r.r.rr.r.rr.r.rr.rrc" "r.D.r.I.r.r.r.rpr.r.r.r.......j."
#define TPLAN "................" "................" "......1....2t2.t" "1.s11.s11s2.t1.t" "1.s11.s11s11t1.t" "1.s11.s11t1.t1s2" "t1t1t1t1t1t2t..." "....2t1s.s.t21.."
constexpr int FG = 512;
constexpr unsigned CODE_TOUCH_MAX = 10240;
constexpr int AUX_NT = 2, AUX_SC1 = 16;

#define WAIT_V(n) asm volatile("s_waitcnt vmcnt(%0)" ::"n"(n) : "memory")
#define WAIT_L(n) asm volatile("s_waitcnt lgkmcnt(%0)" ::"n"(n) : "memory")
#define WAIT_LB(n) __builtin_amdgcn_s_waitcnt(0xC07F | ((n) << 8))
#define BAR() do { asm volatile("" ::: "memory"); __builtin_amdgcn_s_barrier(); asm volatile("" ::: "memory"); } while (0)
#define SCHED() __builtin_amdgcn_sched_barrier(0)
#define TAG_OF(e) ((e) * 0x9E3779B1u + 0x7F4A7C15u)
#define RLX_AGENT __ATOMIC_RELAXED, __HIP_MEMORY_SCOPE_AGENT

__device__ __forceinline__ unsigned pack_bf16(float a, float b) {
  f32x2 v = {a, b};
  bf16x2_t r = __builtin_convertvector(v, bf16x2_t);
  return __builtin_bit_cast(unsigned, r);
}
__device__ __forceinline__ int kstep_of(int s, int tm) {
  const int u = (int)(0xFEDCBA9765432180ull >> (4 * s)) & 15;
  return (u & 8) + ((tm + u) & 7);
}

__device__ __forceinline__ void pin_acc(f32x4& a, int k) {
  switch (k) {
    case 0: asm volatile("" : "+{v[2:5]}"(a)); break;
    case 1: asm volatile("" : "+{v[6:9]}"(a)); break;
    case 2: asm volatile("" : "+{v[10:13]}"(a)); break;
    case 3: asm volatile("" : "+{v[14:17]}"(a)); break;
    case 4: asm volatile("" : "+{v[18:21]}"(a)); break;
    case 5: asm volatile("" : "+{v[22:25]}"(a)); break;
    case 6: asm volatile("" : "+{v[26:29]}"(a)); break;
    case 7: asm volatile("" : "+{v[30:33]}"(a)); break;
    case 8: asm volatile("" : "+{v[34:37]}"(a)); break;
    case 9: asm volatile("" : "+{v[38:41]}"(a)); break;
    case 10: asm volatile("" : "+{v[42:45]}"(a)); break;
    case 11: asm volatile("" : "+{v[46:49]}"(a)); break;
    case 12: asm volatile("" : "+{v[50:53]}"(a)); break;
    case 13: asm volatile("" : "+{v[54:57]}"(a)); break;
    case 14: asm volatile("" : "+{v[58:61]}"(a)); break;
    case 15: asm volatile("" : "+{v[62:65]}"(a)); break;
    case 16: asm volatile("" : "+{v[66:69]}"(a)); break;
    case 17: asm volatile("" : "+{v[70:73]}"(a)); break;
    case 18: asm volatile("" : "+{v[74:77]}"(a)); break;
    case 19: asm volatile("" : "+{v[78:81]}"(a)); break;
    case 20: asm volatile("" : "+{v[82:85]}"(a)); break;
    case 21: asm volatile("" : "+{v[86:89]}"(a)); break;
    case 22: asm volatile("" : "+{v[90:93]}"(a)); break;
    case 23: asm volatile("" : "+{v[94:97]}"(a)); break;
    case 24: asm volatile("" : "+{v[98:101]}"(a)); break;
    case 25: asm volatile("" : "+{v[102:105]}"(a)); break;
    case 26: asm volatile("" : "+{v[106:109]}"(a)); break;
    case 27: asm volatile("" : "+{v[110:113]}"(a)); break;
    case 28: asm volatile("" : "+{v[114:117]}"(a)); break;
    case 29: asm volatile("" : "+{v[118:121]}"(a)); break;
    case 30: asm volatile("" : "+{v[122:125]}"(a)); break;
    case 31: asm volatile("" : "+{v[126:129]}"(a)); break;
  }
}
__device__ __forceinline__ void wait_lb(int n) {
  switch (n) {
    case 0: WAIT_LB(0); break;   case 1: WAIT_LB(1); break;   case 2: WAIT_LB(2); break;   case 3: WAIT_LB(3); break;
    case 4: WAIT_LB(4); break;   case 5: WAIT_LB(5); break;   case 6: WAIT_LB(6); break;   case 7: WAIT_LB(7); break;
    case 8: WAIT_LB(8); break;   case 9: WAIT_LB(9); break;   case 10: WAIT_LB(10); break; case 11: WAIT_LB(11); break;
    case 12: WAIT_LB(12); break; case 13: WAIT_LB(13); break; case 14: WAIT_LB(14); break;
  }
}
struct TailPlan {
  int st[128], pk[129], pj[64], pd[64];
  constexpr TailPlan() : st(), pk(), pj(), pd() {
    for (int k = 0; k < 64; k++) { pj[k] = k < 48 ? k >> 1 : 24 + ((k - 48) >> 3) * 4 + (k & 3); pd[k] = k < 48 ? k & 1 : (k >> 2) & 1; }
    const char* const plan =
        TPLAN;
    int k = 0, j = 0;
    for (int u = 0; u < 128; u++) {
      const char c = plan[u];
      pk[u] = k; st[u] = (c == 's' || c == 't') ? j++ : -1;
      k += c == '2' ? 2 : (c == '1' || c == 't') ? 1 : 0;
    }
    pk[128] = k;
  }
};
constexpr TailPlan tl;
static_assert(tl.pk[128] == 60, "");
struct ReadPlan {
  int rd[64], wt[64], nf[32], n2;
  constexpr ReadPlan() : rd(), wt(), nf(), n2(0) {
    const char* const plan = RPLAN;
    int k = 0;
    for (int u = 0; u < 64 - BI; u++) {
      const char c = plan[u];
      if (u == 32 - BI) n2 = k;
      if (u >= 32 - BI) nf[u - (32 - BI)] = k - n2;
      wt[u] = -1; rd[u] = -1;
      if (c != 'r' && c != '.') {
        const int i = (u - (32 - BI) + ((c | 32) - 'a' + 1)) | 1;
        const int need = i < 1 ? 2 : i < 10 ? i + 3 : i < 16 ? 12 : i < 17 ? 13 : i < 24 ? 14 : i < 25 ? 15 : i < 32 ? 16 : i < 40 ? i - 15 : 24;
        wt[u] = k - need;
      }
      if (c == 'r' || (c >= 'A' && c <= 'Z')) rd[u] = k++;
    }
    for (int u = 64 - BI; u < 64; u++) { rd[u] = -1; wt[u] = -1; }
    if (k != 24) n2 = -1;
  }
};
constexpr ReadPlan rp;
static_assert(rp.n2 >= 12 && rp.wt[31 - BI] >= 0, "");
struct QRegs { i32x4 r[8]; };
struct LV { f32x2 v[4][4]; float sc[4]; };
__device__ __forceinline__ f32x2 pair_lookup(const char* lut, int q0, int q1) { return *(const f32x2*)(lut + (q0 << 3) + (q1 << 7)); }
__device__ __forceinline__ u32x4 q_pack(const LV& l, int i) {
  u32x4 o;
  o.x = pack_bf16(l.v[i][0].x * l.sc[i], l.v[i][0].y * l.sc[i]); o.y = pack_bf16(l.v[i][1].x * l.sc[i], l.v[i][1].y * l.sc[i]);
  o.z = pack_bf16(l.v[i][2].x * l.sc[i], l.v[i][2].y * l.sc[i]); o.w = pack_bf16(l.v[i][3].x * l.sc[i], l.v[i][3].y * l.sc[i]);
  return o;
}

__global__ __launch_bounds__(512, 2) void nf4_gemm(const uint16_t* x_, const int* qw_, const uint16_t* cb_, const uint16_t* am_,
                                                   char* ws_, uint16_t* out_) {
  __shared__ __attribute__((aligned(1024))) char shm_raw[SHM_BYTES];
  char* const shm = shm_raw + HDR;
  u32x4 rsQ; u64x2 k2, k4; unsigned long long k0;
  asm volatile("s_load_dwordx2 s[20:21], %4, 8\n\ts_load_dwordx4 %1, %4, 16\n\ts_load_dwordx4 %2, %4, 32\n\ts_load_dwordx2 %3, %4, 0\n\t"
               "s_mov_b32 s22, %5\n\ts_mov_b32 s23, 0x20000"
               : "=&{s[20:23]}"(rsQ), "=&{s[8:11]}"(k2), "=&{s[16:19]}"(k4), "=&{s[4:5]}"(k0)
               : "s"(__builtin_amdgcn_kernarg_segment_ptr()), "n"(K_ * N_ * 4));
#define ARGS() asm volatile("s_waitcnt lgkmcnt(0)" : "+{s[20:23]}"(rsQ), "+{s[8:11]}"(k2), "+{s[16:19]}"(k4), "+{s[4:5]}"(k0))
#define PTRS() asm volatile("" : "+s"(k2), "+s"(k4), "+s"(k0));                                                                    \
  const char* const x = (const char*)k0; const char* const am = (const char*)k2.y; char* const ws = (char*)k4.x;                  \
  gu32* const flags = (gu32*)(ws + SYNC_OFF + (size_t)tn * FG);                                                                   \
  const __amdgpu_buffer_rsrc_t rsW = __builtin_amdgcn_make_buffer_rsrc((void*)ws, (short)0, (int)WIMG_BYTES, 0x00020000);         \
  (void)x; (void)am; (void)flags; (void)rsW
  asm volatile("s_cmp_lt_u32 %0, 32\n\ts_cbranch_scc1 .Lnf4_prefetch" :: "{s2}"(blockIdx.x), "{v0}"(threadIdx.x), "{s[4:5]}"(k0) : "scc");
  unsigned long long pc0; asm volatile("s_getpc_b64 %0" : "=s"(pc0));
  unsigned ct;
  asm volatile("global_load_dword %0, %1, %2" : "=&v"(ct) : "v"(min((unsigned)(threadIdx.x * 128u), CODE_TOUCH_MAX)), "s"(pc0) : "memory");
  const int tm = (blockIdx.x & 6) + ((blockIdx.x >> 3) & 1), tn = ((blockIdx.x >> 3) & 30) - 4 + (blockIdx.x & 1);
#define TILE_OFF(ks) (((ks) * 32 + (tn >> 1) + (tn & 1) * 16) * (2 * WB))
  const int rot = tm;
  const int tid = threadIdx.x, lane = tid & 63;
  const int wid = __builtin_amdgcn_readfirstlane(tid >> 6);
  const int lc0 = wid < 4 ? wid : wid & 1 ? 0 : 3;
#define PART(lc) (((lc) >> 1) * 1024 + ((lc) & 1) * 128)
  const int myks = wid < 4 ? rot : rot + 8;
  unsigned qvoff = (unsigned)(((lane >> 3) * 8 * N_ + (lane & 7) * 4) * 4);
  unsigned avoff = (unsigned)((myks * N_ + tn * BN) * 2 + lc0 * 64 + (lane & 7) * 8);
  unsigned evoff = (unsigned)(SYNC_OFF + tn * FG + 256);
  unsigned qso = (unsigned)(tn * BN * 4 + myks * (BK * N_ * 4) + lc0 * 128);
  unsigned co0 = (unsigned)((lane & 15) * 2), co1 = (unsigned)((tid >> 4) * 2);
  asm volatile("" : "+v"(qvoff), "+v"(avoff), "+v"(evoff), "+s"(qso), "+v"(co0), "+v"(co1));
  SCHED();
  ARGS();
  char* lut = shm + LUT_OFF;
#define Q_ROW(Q, j, so_) asm volatile("buffer_load_dwordx4 %0, %1, %2, %3 offen nt" : "=&v"(Q.r[j]) : "v"(qvoff), "s"(rsQ), "s"((so_) + (j) * (N_ * 4)) : "memory")
#define Q_ROWS(Q, so_) do { _Pragma("unroll") for (int j_ = 0; j_ < 8; j_++) Q_ROW(Q, j_, so_); } while (0)
#define Q_SC(S) asm volatile("global_load_dwordx2 %0, %1, %2" : "=&v"(S) : "v"(avoff), "s"(k2.y) : "memory")
#define TAG_LOAD(T) asm volatile("global_load_dword %0, %1, %2 sc1" : "=&v"(T) : "v"(evoff), "s"(k4.x) : "memory")
#define Q_PIN(q) asm volatile("" : "+v"(q.r[0]), "+v"(q.r[1]), "+v"(q.r[2]), "+v"(q.r[3]), "+v"(q.r[4]), "+v"(q.r[5]), "+v"(q.r[6]), "+v"(q.r[7]))
#define SCALES(S) do { asm volatile("" : "+v"(S));                                                                                \
      lv.sc[0] = __builtin_bit_cast(float, S.x << 16); lv.sc[1] = __builtin_bit_cast(float, S.x & 0xffff0000u);                   \
      lv.sc[2] = __builtin_bit_cast(float, S.y << 16); lv.sc[3] = __builtin_bit_cast(float, S.y & 0xffff0000u); } while (0)
#define ROUND(Q, dst, PUB, g_) do { Q_PIN(Q);                                                                                     \
      _Pragma("unroll") for (int i_ = 0; i_ < 4; i_++) _Pragma("unroll") for (int a_ = 0; a_ < 4; a_++)                           \
        lv.v[i_][a_] = pair_lookup(lut, Q.r[2 * a_][i_], Q.r[2 * a_ + 1][i_]);                                                    \
      SCHED();                                                                                                                    \
      char* d_ = shm + (dst) + io;                                                                                                \
      _Pragma("unroll") for (int i_ = 0; i_ < 4; i_++) { const u32x4 o_ = q_pack(lv, i_);                                         \
        if (PUB) { __builtin_amdgcn_raw_buffer_store_b128(o_, rsW, (g_) + i_ * 256, 0, AUX_SC1); SCHED(); }                       \
        *(u32x4*)(d_ + i_ * 256) = o_; }                                                                                          \
      SCHED(); } while (0)
  LV lv;

  if (__builtin_expect(wid < 4, 1)) {
    QRegs q; u32x2 qsv; unsigned c0, c1, tagv;
    asm volatile("global_load_ushort %0, %2, %4\n\tglobal_load_ushort %1, %3, %4" : "=&v"(c0), "=&v"(c1) : "v"(co0), "v"(co1), "s"(k2.x) : "memory");
    Q_ROWS(q, qso);
    Q_SC(qsv);
    TAG_LOAD(tagv);
    SCHED();
    PTRS();
    BAR();
    const int io = (lane >> 3) * KCB + (lane & 7) * 16;
    unsigned gA = (unsigned)(TILE_OFF(rot) + (PART(lc0) + io));
    asm volatile("" : "+v"(gA));
    f32x4 acc[4][8];
#pragma unroll
    for (int m = 0; m < 4; m++)
#pragma unroll
      for (int n = 0; n < 8; n++) { acc[m][n] = (f32x4){0.f, 0.f, 0.f, 0.f}; pin_acc(acc[m][n], m * 8 + n); }
    WAIT_V(10);
    asm volatile("" : "+v"(c0), "+v"(c1)); asm volatile("" :: "v"(ct));
    *(u32x2*)(lut + tid * 8) = (u32x2){c0 << 16, c1 << 16};
    WAIT_L(0);
    BAR();
    WAIT_V(0);
    SCALES(qsv);
    ROUND(q, XB + PART(lc0), 1, gA);
    BAR();
    SCHED();
    const int fr = lane & 15, fq = lane >> 4;
    const int xo0 = (wid * 64 + fr) * 128 + ((fq ^ (fr >> 1)) << 4), xo1 = xo0 ^ 64;
    const int wo = XB + fq * KCB + (fr << 4);
    bf16x8 xl[4], xh[4], wa[8], wb[8];
    typedef __attribute__((address_space(3))) const bf16x8 lds_frag;
#define LD(a_) (*(lds_frag*)(a_))
#define MF(m_, n_, X, W) do { acc[m_][n_] = __builtin_amdgcn_mfma_f32_16x16x32_bf16(X, W, acc[m_][n_], 0, 0, 0); pin_acc(acc[m_][n_], (m_) * 8 + (n_)); } while (0)
#define RW(wf, j_, W_, cb) wf[j_] = LD((W_) + ((j_) & 1) * (4 * KCB) + ((cb) * 4 + ((j_) >> 1)) * 256)
#define RX(xf, j_, A, B, off) xf[j_] = LD((((j_) & 1) ? (B) : (A)) + (off) + ((j_) >> 1) * 2048)
#define KH(i) (((i) ^ ((i) >> 1)) & 1)
#define MP(m0, cb, i, X, W) MF((m0) + (((i) >> 3) & 1), (cb) * 4 + (((i) >> 1) & 3), X[(((i) >> 2) & 2) + KH(i)], W[((i) & 6) + KH(i)])
#define VADD(d, v_, s_) asm volatile("v_add_u32 %0, %1, %2" : "=v"(d) : "s"(s_), "v"(v_))
    const unsigned lb = (unsigned)(size_t)shm;
    unsigned bW = lb + wo, bA = lb + xo0, bB = lb + xo1;
    unsigned vf = (unsigned)(((tm * BM + wid * 64 + 4 * fq) * N_ + tn * BN) * 2 + fr * 8);
    asm volatile("" : "+v"(bW), "+v"(bA), "+v"(bB), "+v"(vf));
    __amdgpu_buffer_rsrc_t rsO = __builtin_amdgcn_make_buffer_rsrc((void*)k4.y, (short)0, M_ * N_ * 2, 0x00020000);
    asm volatile("" : "+s"(rsO));
    unsigned ro[16];
#pragma unroll
    for (int j = 0; j < 16; j++) { ro[j] = (unsigned)(((j >> 2) * 16 + (j & 3)) * (N_ * 2)); asm volatile("" : "+s"(ro[j])); }
    WAIT_V(0);
    asm volatile("" : "+v"(tagv));
    if (lane == 0) __hip_atomic_store(flags + (rot * 4 + wid), TAG_OF(tagv), RLX_AGENT);
    unsigned aW = bW, aA = bA, aB = bB;
    asm volatile("" : "+v"(aW), "+v"(aA), "+v"(aB));
    unsigned so_ = 0;
    WAIT_L(0);
    SCHED();
    BAR();
#define RK(k) do { if (k == 0 || k == 2) RX(xl, (k >> 1) & 1, aA, aB, 0); else if (k == 1) RW(wa, 0, aW, 0);                       \
      else if (k < 10) RW(wa, (k - 2) & 7, aW, 0); else if (k < 12) RX(xl, (k - 8) & 3, aA, aB, 0);                               \
      else if (k < 16) RX(xh, k & 3, aA, aB, 4096); else RW(wb, k & 7, aW, 1); } while (0)
#pragma unroll
    for (int k = 0; k < rp.n2; k++) { RK(k); SCHED(); }
    asm volatile(".p2align 5\n\t.rept %0\n\ts_nop 0\n\t.endr" :: "n"(6));
    SCHED();
    WAIT_LB(rp.wt[31 - BI]);
    SCHED();
#pragma unroll 1
    for (int t = 0; t < NT - 2;) {
#pragma unroll
      for (int i = 0; i < 32; i++) {
        if (i < 16) MP(0, 0, i, xl, wa); else MP(2, 0, i, xh, wa);
        SCHED();
        wait_lb(rp.wt[32 - BI + i]);
        if (rp.rd[32 - BI + i] >= 0) RK(rp.rd[32 - BI + i]);
        SCHED();
      }
#pragma unroll
      for (int i = 0; i < 32; i++) {
        if (i == 31) { WAIT_LB(rp.wt[31 - BI]); SCHED(); }
        if (i < 16) MP(0, 1, i, xl, wb); else MP(2, 1, i, xh, wb);
        SCHED();
        if (i == 0) { so_ = so_ == 2 * SB ? 0 : so_ + SB; asm volatile("" : "+s"(so_)); }
        if (i == 3) VADD(aW, bW, so_);
        if (i == 5) VADD(aA, bA, so_);
        if (i == 7) VADD(aB, bB, so_);
        if (i == 4) { t++; asm volatile("" : "+s"(t)); }
        if (i == BI - 1) BAR();
        if (i >= BI && rp.rd[(i - BI) & 63] >= 0) RK(rp.rd[(i - BI) & 63]);
        SCHED();
      }
    }
    u32x2 ev[32];
    bf16x8 wc[8], xc[4];
#define PC_M(j) (((j) >> 4) * 2 + (((j) >> 2) & 1))
#define PC_C(j) ((((j) >> 3) ^ ((j) >> 4)) & 1)
#define PK(j, d) ev[j][d] = pack_bf16(acc[PC_M(j)][PC_C(j) * 4 + 2 * (d)][(j) & 3], acc[PC_M(j)][PC_C(j) * 4 + 2 * (d) + 1][(j) & 3])
#define OUT_ST(j) __builtin_amdgcn_raw_buffer_store_b64(ev[j], rsO, vf + PC_C(j) * 128, ro[PC_M(j) * 4 + ((j) & 3)], AUX_NT)
#define PH(p, m0, cb, XC, WC, RD) do { _Pragma("unroll") for (int i = 0; i < 16; i++) {                                           \
      MP(m0, cb, i, XC, WC); SCHED();                                                                                             \
      OUTW(p * 16 + i, RD(i)); } } while (0)
#define OUTW(u, R_) do {                                                                                                          \
      if (tl.st[u] >= 0) { OUT_ST(tl.st[u] & 31); SCHED(); }                                                                      \
      R_; SCHED();                                                                                                                \
      if (tl.pk[(u) + 1] > tl.pk[u]) PK(tl.pj[tl.pk[u]], tl.pd[tl.pk[u]]);                                                        \
      if (tl.pk[(u) + 1] > tl.pk[u] + 1) PK(tl.pj[tl.pk[u] + 1], tl.pd[tl.pk[u] + 1]);                                            \
      SCHED();                                                                                                                    \
      if (tl.st[u] >= 0) asm volatile("" :: "v"(ev[tl.st[u] & 31])); } while (0)
#define RDS(i, WN, W_, cb, XN, xA, xB, xo, x0, nx0, nx1, w14) do {                                                                \
      if (i == 5) WAIT_LB(5);                                                                                                     \
      if (i == 14) WAIT_LB(w14);                                                                                                  \
      if (i < nx0) RX(XN, (x0 + i) & 3, xA, xB, xo);                                                                              \
      if (i >= nx0 && i < nx0 + 8) RW(WN, (i - nx0) & 7, W_, cb);                                                                 \
      if (i >= nx0 + 8 && i < nx0 + nx1 + 8) RX(XN, (x0 + i - 8) & 3, xA, xB, xo); } while (0)
#define R0(i) do { if (i < 5 && rp.wt[32 - BI + i] >= 0) wait_lb(rp.wt[32 - BI + i] - rp.nf[i] + i);                              \
      RDS(i, wb, aW, 1, xh, aA, aB, 0, 0, 0, 0, 0); } while (0)
#define R1(i) RDS(i, wa, bW, 0, xh, bA, bB, 0, 0, 2, 2, 3)
#define R2(i) RDS(i, wb, bW, 1, xc, bA, bB, 4096, 0, 0, 2, 2)
#define R3(i) RDS(i, wc, aW, 1, xc, bA, bB, 4096, 2, 2, 0, 8)
#define R4(i) RDS(i, wa, aW, 0, xl, aA, aB, 4096, 0, 4, 0, 8)
#define R5(i) RDS(i, wb, bW, 0, xl, aA, aB, 0, 0, 0, 0, 0)
    PH(0, 0, 0, xl, wa, R0);
    BAR();
    SCHED();
    PH(1, 0, 1, xl, wb, R1);
    PH(2, 0, 0, xh, wa, R2);
    PH(3, 0, 1, xh, wb, R3);
    PH(4, 2, 1, xc, wb, R4);
    PH(5, 2, 1, xl, wc, R5);
#define PL(m) do { _Pragma("unroll") for (int i = 0; i < 16; i++) {                                                               \
      const int e = (m - 2) * 2 + (i & 1), f = ((i >> 1) & 6) + (i & 1);                                                          \
      if (i & 2) MF(m, i >> 2, xc[e], wb[f]); else MF(m, i >> 2, xl[e], wa[f]);                                                   \
      SCHED();                                                                                                                    \
      OUTW(64 + m * 16 + i, (void)0); } } while (0)
    PL(2);
    PL(3);
#pragma unroll
    for (int j = 28; j < 32; j++) { PK(j, 1); SCHED(); OUT_ST(j); SCHED(); }
  } else {
  PTRS();
  if (wid < 6) {
    const int h = wid - 4;
    unsigned xv[16];
#pragma unroll
    for (int i = 0; i < 16; i++) xv[i] = (unsigned)((8 * i + (lane >> 3)) * (K_ * 2) + (((lane & 7) ^ ((i & 1) * 4 + (lane >> 4))) << 4));
    const char* xbase = x + (size_t)(tm * BM + h * 128) * (K_ * 2);
    const unsigned ldsx = (unsigned)(size_t)(shm) + h * 16384;
#define X8(o, l_, b_)                                                                                                             \
      asm volatile("s_mov_b32 m0, %8\n\ts_nop 0\n\tglobal_load_lds_dwordx4 %0, %9\n\t"                                            \
                   "s_add_u32 m0, m0, 0x400\n\ts_nop 0\n\tglobal_load_lds_dwordx4 %1, %9\n\t"                                     \
                   "s_add_u32 m0, m0, 0x400\n\ts_nop 0\n\tglobal_load_lds_dwordx4 %2, %9\n\t"                                     \
                   "s_add_u32 m0, m0, 0x400\n\ts_nop 0\n\tglobal_load_lds_dwordx4 %3, %9\n\t"                                     \
                   "s_add_u32 m0, m0, 0x400\n\ts_nop 0\n\tglobal_load_lds_dwordx4 %4, %9\n\t"                                     \
                   "s_add_u32 m0, m0, 0x400\n\ts_nop 0\n\tglobal_load_lds_dwordx4 %5, %9\n\t"                                     \
                   "s_add_u32 m0, m0, 0x400\n\ts_nop 0\n\tglobal_load_lds_dwordx4 %6, %9\n\t"                                     \
                   "s_add_u32 m0, m0, 0x400\n\ts_nop 0\n\tglobal_load_lds_dwordx4 %7, %9"                                         \
                   :: "v"(xv[o]), "v"(xv[o + 1]), "v"(xv[o + 2]), "v"(xv[o + 3]), "v"(xv[o + 4]), "v"(xv[o + 5]), "v"(xv[o + 6]), "v"(xv[o + 7]), \
                      "s"(l_), "s"(b_) : "memory", "scc")
#define ISSUE_X(s, slot) do { const char* b_ = xbase + kstep_of(s, tm) * 128; const unsigned l_ = ldsx + (slot) * SB;       \
      X8(0, l_, b_); X8(8, l_ + 8192, b_); } while (0)
    BAR();
    BAR();
    BAR();
    ISSUE_X(0, 0);
    ISSUE_X(1, 1);
    WAIT_V(16);
    asm volatile("" :: "v"(ct));
    BAR();
    int xslot = 2;
#pragma unroll 1
    for (int t = -1; t < NT - 2; t++) {
      if (t + 3 < NT) { ISSUE_X(t + 3, xslot); xslot = xslot == NS - 1 ? 0 : xslot + 1; WAIT_V(16); } else { WAIT_V(0); }
      BAR();
    }
  } else {
    QRegs qa, qb; u32x2 sb, sb2; unsigned tagv;
    Q_SC(sb); avoff ^= 64; Q_SC(sb2); TAG_LOAD(tagv);
    unsigned qso2 = qso ^ 128;
    asm volatile("" : "+s"(qso2));
    const int io = (lane >> 3) * KCB + (lane & 7) * 16;
    const int fks = kstep_of(lane >> 2, tm);
    unsigned fvo = (unsigned)(fks * 16 + (fks < 8 ? (lane & 3) * 4 : 0));
    asm volatile("" : "+v"(fvo));
    BAR();
    Q_ROWS(qa, qso);
    Q_ROWS(qb, qso2);
    BAR();
    BAR();
    WAIT_V(8);
    asm volatile("" :: "v"(ct));
    SCALES(sb);
    ROUND(qa, STG_OFF + PART(lc0), 0, 0);
    WAIT_V(0);
    unsigned fv = tagv;
#define POLL(f_, m_, b_) asm volatile("s_mov_b64 exec, %3\n\tglobal_load_dword %0, %1, %2 sc1\n\ts_mov_b64 exec, -1" : "+v"(f_) : "v"(fvo), "s"(b_), "s"(m_) : "memory")
    SCALES(sb2);
    ROUND(qb, STG_OFF + PART(lc0 ^ 1), 0, 0);
    WAIT_L(0);
    asm volatile("" : "+v"(tagv));
    const unsigned tag = TAG_OF(tagv);
   if (wid == 6) {
    __builtin_amdgcn_s_setprio(3);
    const unsigned wv = (unsigned)(lane * 16), wv2 = wv + 4096, wv3 = wv + 8192, wv4 = wv + 12288;
    const unsigned ldsw = (unsigned)(size_t)(shm) + XB;
#define ISSUE_W(s, slot) do { const char* b_ = ws + (unsigned)TILE_OFF(kstep_of(s, tm)); const unsigned l_ = ldsw + (slot) * SB;  \
      asm volatile("s_mov_b32 m0, %4\n\ts_nop 0\n\t"                                                                              \
                   "global_load_lds_dwordx4 %0, %5 sc1\n\tglobal_load_lds_dwordx4 %0, %5 offset:1024 sc1\n\t"                     \
                   "global_load_lds_dwordx4 %0, %5 offset:2048 sc1\n\tglobal_load_lds_dwordx4 %0, %5 offset:3072 sc1\n\t"         \
                   "s_add_u32 m0, m0, 0x1000\n\ts_nop 0\n\t"                                                                      \
                   "global_load_lds_dwordx4 %1, %5 sc1\n\tglobal_load_lds_dwordx4 %1, %5 offset:1024 sc1\n\t"                     \
                   "global_load_lds_dwordx4 %1, %5 offset:2048 sc1\n\tglobal_load_lds_dwordx4 %1, %5 offset:3072 sc1\n\t"         \
                   "s_add_u32 m0, m0, 0x1000\n\ts_nop 0\n\t"                                                                      \
                   "global_load_lds_dwordx4 %2, %5 sc1\n\tglobal_load_lds_dwordx4 %2, %5 offset:1024 sc1\n\t"                     \
                   "global_load_lds_dwordx4 %2, %5 offset:2048 sc1\n\tglobal_load_lds_dwordx4 %2, %5 offset:3072 sc1\n\t"         \
                   "s_add_u32 m0, m0, 0x1000\n\ts_nop 0\n\t"                                                                      \
                   "global_load_lds_dwordx4 %3, %5 sc1\n\tglobal_load_lds_dwordx4 %3, %5 offset:1024 sc1\n\t"                     \
                   "global_load_lds_dwordx4 %3, %5 offset:2048 sc1\n\tglobal_load_lds_dwordx4 %3, %5 offset:3072 sc1"             \
                   :: "v"(wv), "v"(wv2), "v"(wv3), "v"(wv4), "s"(l_), "s"(b_) : "memory", "scc"); } while (0)
#define POLL_BLOCK(m_) do { unsigned f_ = ~tag; POLL(f_, m_, flags); asm volatile("s_waitcnt vmcnt(0)" : "+v"(f_) :: "memory"); rdy |= __ballot(f_ == tag); } while (0)
#define READY(s) (((unsigned)(rdy >> (4 * (s))) & 15u) == 15u)
#define NEXT_W() do { nw++; wslot = wslot == NS - 1 ? 0 : wslot + 1; } while (0)
    unsigned long long rdy = 0, want = 0xff00ull;
    unsigned spins = 0;
    BAR();
    int nw = 2, wslot = 2;
    bool preg = false;
#pragma unroll 1
    for (int t = -1; t < NT - 2; t++) {
      const int lim = t + 3 < NT ? t + 3 : NT - 1;
      if (preg) { WAIT_V(16); } else { WAIT_V(0); }
      asm volatile("" : "+v"(fv));
      rdy |= __ballot(fv == tag);
      if (nw <= t + 2) {
#pragma unroll 1
        while (!READY(nw)) { POLL_BLOCK((0xffull << (4 * nw)) & ~rdy); if (++spins > SPIN_MAX) rdy = ~0ull; }
        ISSUE_W(nw, wslot); NEXT_W();
      }
      if (t == 0) want = 0xfffffff00ull;
      if (t == POLLB_T) want = ~0xffull;
      const unsigned long long m = want & ~rdy;
      POLL(fv, m ? m : 1ull, m ? (const char*)flags : (const char*)am);
      preg = nw <= lim && READY(nw);
      if (preg) { ISSUE_W(nw, wslot); NEXT_W(); WAIT_V(17); } else { WAIT_V(1); }
      BAR();
    }
    WAIT_V(0);
    asm volatile("" : "+v"(fv));
    if (tm == 0 && lane == 0) __hip_atomic_store(flags + 64, tag, RLX_AGENT);
   } else {
    BAR();
    u32x4 bv[16];
#pragma unroll
    for (int i = 0; i < 16; i++) bv[i] = *(const u32x4*)(shm + STG_OFF + lane * 16 + i * 1024);
    WAIT_L(0);
    SCHED();
    const unsigned g_ = (unsigned)(TILE_OFF(rot + 8) + lane * 16);
#pragma unroll
    for (int t = 0; t <= 4; t++) {
      BAR();
      if (t == 1 || t == 2) {
        __builtin_amdgcn_s_sleep(10);
#pragma unroll
        for (int i = (t - 1) * 8; i < t * 8; i++) __builtin_amdgcn_raw_buffer_store_b128(bv[i], rsW, g_ + i * 1024, 0, AUX_SC1);
        SCHED();
      }
    }
    WAIT_V(0);
    asm volatile("" :: "v"(fv));
    if (lane == 0) __hip_atomic_store(flags + (rot + 8) * 4, tag, RLX_AGENT);
#pragma unroll 1
    for (int t = 5; t < NT - 1; t++) BAR();
   }
  }
  }
  asm volatile("s_endpgm\n.Lnf4_prefetch:\n\t"
               "s_and_b32 s3, s2, 6\n\ts_bfe_u32 s0, s2, 0x10003\n\ts_or_b32 s3, s3, s0\n\t"
               "v_lshlrev_b32 v2, 10, v0\n\tv_and_b32 v2, 0xfffff800, v2\n\tv_and_b32 v3, 1, v0\n\tv_lshl_or_b32 v2, v3, 6, v2\n\t"
               "v_lshl_or_b32 v2, s3, 19, v2\n\t"
               "s_mov_b32 s1, 0xfdb96428\n\ts_cmp_lt_u32 s2, 16\n\ts_cmov_b32 s1, 0xeca75310\n\ts_mov_b32 s2, %1\n\ts_waitcnt lgkmcnt(0)\n\t"
               "s_cmp_eq_u32 s3, 3\n\ts_cbranch_scc0 3f\n\ts_sleep 1\n\ts_add_u32 s2, s2, 3\n\ts_branch 1f\n3:\n\ts_sleep 44\n"
               "1:\n\ts_add_u32 s0, s1, s3\n\ts_xor_b32 s0, s0, s1\n\ts_and_b32 s0, s0, 7\n\ts_xor_b32 s0, s0, s1\n\ts_and_b32 s0, s0, 15\n\t"
               "v_lshl_add_u32 v3, s0, 7, v2\n\tglobal_load_dword v4, v3, s[4:5]\n"
               "2:\n\ts_sleep %0\n\ts_sub_u32 s2, s2, 1\n\ts_cbranch_scc0 2b\n\ts_mov_b32 s2, 0\n\t"
               "s_lshr_b32 s1, s1, 4\n\ts_cbranch_scc1 1b\n\ts_waitcnt vmcnt(0)"
               :: "n"(12), "n"(2) : "memory");
}

extern "C" void kernel_launch(void* const* d_in, const int* in_sizes, int n_in, void* d_out, int out_size, void* d_ws,
                              size_t ws_size, hipStream_t stream) {
  nf4_gemm<<<256, 512, 0, stream>>>((const uint16_t*)d_in[0], (const int*)d_in[1], (const uint16_t*)d_in[2],
                                   (const uint16_t*)d_in[3], (char*)d_ws, (uint16_t*)d_out);
}
```

```cpp
#include <hip/hip_runtime.h>
#include <hip/hip_bf16.h>
#include <stdint.h>

typedef __attribute__((ext_vector_type(8))) short bf16x8;
typedef __attribute__((ext_vector_type(4))) float f32x4;
typedef __attribute__((ext_vector_type(4))) int i32x4;
typedef __attribute__((ext_vector_type(4))) unsigned u32x4;
typedef __attribute__((ext_vector_type(2))) unsigned u32x2;
typedef __attribute__((ext_vector_type(2))) unsigned long long u64x2;
typedef __attribute__((ext_vector_type(2))) float f32x2;
typedef __attribute__((ext_vector_type(2))) __bf16 bf16x2_t;
typedef __attribute__((address_space(1))) unsigned gu32;

constexpr int M_ = 2048, K_ = 1024, N_ = 3584;
constexpr int BM = 256, BN = 128, BK = 64, NT = K_ / BK;
constexpr int XB = BM * BK * 2;
constexpr int WB = BN * BK * 2;
constexpr int KCB = BN * 16;
constexpr int SB = XB + WB;
constexpr int NS = 3;
constexpr int HDR = 2048;
constexpr int LUT_OFF = -HDR;
constexpr int STG_OFF = SB + XB;
constexpr int SHM_BYTES = HDR + NS * SB;
constexpr size_t WIMG_BYTES = (size_t)16 << 20;
constexpr size_t SYNC_OFF = WIMG_BYTES;
constexpr unsigned SPIN_MAX = 1u << 17;
constexpr int POLLB_T = 5;
constexpr int BI = 12;
#define RPLAN "r.r.rr.r.rr.r.rr.rrc" "r.D.r.I.r.r.r.rpr.r.r.r.......j."
#define TPLAN "................" "................" "......1....2t2.t" "1.s11.s11s2.t1.t" "1.s11.s11s11t1.t" "1.s11.s11t1.t1s2" "t1t1t1t1t1t2t..." "....2t1s.s.t21.."
constexpr int FG = 512;
constexpr unsigned CODE_TOUCH_MAX = 10240;
constexpr int AUX_NT = 2, AUX_SC1 = 16;

#define WAIT_V(n) asm volatile("s_waitcnt vmcnt(%0)" ::"n"(n) : "memory")
#define WAIT_L(n) asm volatile("s_waitcnt lgkmcnt(%0)" ::"n"(n) : "memory")
#define WAIT_LB(n) __builtin_amdgcn_s_waitcnt(0xC07F | ((n) << 8))
#define BAR() do { asm volatile("" ::: "memory"); __builtin_amdgcn_s_barrier(); asm volatile("" ::: "memory"); } while (0)
#define SCHED() __builtin_amdgcn_sched_barrier(0)
#define TAG_OF(e) ((e) * 0x9E3779B1u + 0x7F4A7C15u)
#define RLX_AGENT __ATOMIC_RELAXED, __HIP_MEMORY_SCOPE_AGENT

__device__ __forceinline__ unsigned pack_bf16(float a, float b) {
  f32x2 v = {a, b};
  bf16x2_t r = __builtin_convertvector(v, bf16x2_t);
  return __builtin_bit_cast(unsigned, r);
}
__device__ __forceinline__ int kstep_of(int s, int tm) { return ((int)(0x9FEDCBA176543280ull >> (4 * s)) & 15) ^ tm; }

__device__ __forceinline__ void pin_acc(f32x4& a, int k) {
  switch (k) {
    case 0: asm volatile("" : "+{v[2:5]}"(a)); break;
    case 1: asm volatile("" : "+{v[6:9]}"(a)); break;
    case 2: asm volatile("" : "+{v[10:13]}"(a)); break;
    case 3: asm volatile("" : "+{v[14:17]}"(a)); break;
    case 4: asm volatile("" : "+{v[18:21]}"(a)); break;
    case 5: asm volatile("" : "+{v[22:25]}"(a)); break;
    case 6: asm volatile("" : "+{v[26:29]}"(a)); break;
    case 7: asm volatile("" : "+{v[30:33]}"(a)); break;
    case 8: asm volatile("" : "+{v[34:37]}"(a)); break;
    case 9: asm volatile("" : "+{v[38:41]}"(a)); break;
    case 10: asm volatile("" : "+{v[42:45]}"(a)); break;
    case 11: asm volatile("" : "+{v[46:49]}"(a)); break;
    case 12: asm volatile("" : "+{v[50:53]}"(a)); break;
    case 13: asm volatile("" : "+{v[54:57]}"(a)); break;
    case 14: asm volatile("" : "+{v[58:61]}"(a)); break;
    case 15: asm volatile("" : "+{v[62:65]}"(a)); break;
    case 16: asm volatile("" : "+{v[66:69]}"(a)); break;
    case 17: asm volatile("" : "+{v[70:73]}"(a)); break;
    case 18: asm volatile("" : "+{v[74:77]}"(a)); break;
    case 19: asm volatile("" : "+{v[78:81]}"(a)); break;
    case 20: asm volatile("" : "+{v[82:85]}"(a)); break;
    case 21: asm volatile("" : "+{v[86:89]}"(a)); break;
    case 22: asm volatile("" : "+{v[90:93]}"(a)); break;
    case 23: asm volatile("" : "+{v[94:97]}"(a)); break;
    case 24: asm volatile("" : "+{v[98:101]}"(a)); break;
    case 25: asm volatile("" : "+{v[102:105]}"(a)); break;
    case 26: asm volatile("" : "+{v[106:109]}"(a)); break;
    case 27: asm volatile("" : "+{v[110:113]}"(a)); break;
    case 28: asm volatile("" : "+{v[114:117]}"(a)); break;
    case 29: asm volatile("" : "+{v[118:121]}"(a)); break;
    case 30: asm volatile("" : "+{v[122:125]}"(a)); break;
    case 31: asm volatile("" : "+{v[126:129]}"(a)); break;
  }
}
__device__ __forceinline__ void wait_lb(int n) {
  switch (n) {
    case 0: WAIT_LB(0); break;   case 1: WAIT_LB(1); break;   case 2: WAIT_LB(2); break;   case 3: WAIT_LB(3); break;
    case 4: WAIT_LB(4); break;   case 5: WAIT_LB(5); break;   case 6: WAIT_LB(6); break;   case 7: WAIT_LB(7); break;
    case 8: WAIT_LB(8); break;   case 9: WAIT_LB(9); break;   case 10: WAIT_LB(10); break; case 11: WAIT_LB(11); break;
    case 12: WAIT_LB(12); break; case 13: WAIT_LB(13); break; case 14: WAIT_LB(14); break;
  }
}
struct TailPlan {
  int st[128], pk[129], pj[64], pd[64];
  constexpr TailPlan() : st(), pk(), pj(), pd() {
    for (int k = 0; k < 64; k++) { pj[k] = k < 48 ? k >> 1 : 24 + ((k - 48) >> 3) * 4 + (k & 3); pd[k] = k < 48 ? k & 1 : (k >> 2) & 1; }
    const char* const plan =
        TPLAN;
    int k = 0, j = 0;
    for (int u = 0; u < 128; u++) {
      const char c = plan[u];
      pk[u] = k; st[u] = (c == 's' || c == 't') ? j++ : -1;
      k += c == '2' ? 2 : (c == '1' || c == 't') ? 1 : 0;
    }
    pk[128] = k;
  }
};
constexpr TailPlan tl;
static_assert(tl.pk[128] == 60, "");
struct ReadPlan {
  int rd[64], wt[64], nf[32], n2;
  constexpr ReadPlan() : rd(), wt(), nf(), n2(0) {
    const char* const plan = RPLAN;
    int k = 0;
    for (int u = 0; u < 64 - BI; u++) {
      const char c = plan[u];
      if (u == 32 - BI) n2 = k;
      if (u >= 32 - BI) nf[u - (32 - BI)] = k - n2;
      wt[u] = -1; rd[u] = -1;
      if (c != 'r' && c != '.') {
        const int i = (u - (32 - BI) + ((c | 32) - 'a' + 1)) | 1;
        const int need = i < 1 ? 2 : i < 10 ? i + 3 : i < 16 ? 12 : i < 17 ? 13 : i < 24 ? 14 : i < 25 ? 15 : i < 32 ? 16 : i < 40 ? i - 15 : 24;
        wt[u] = k - need;
      }
      if (c == 'r' || (c >= 'A' && c <= 'Z')) rd[u] = k++;
    }
    for (int u = 64 - BI; u < 64; u++) { rd[u] = -1; wt[u] = -1; }
    if (k != 24) n2 = -1;
  }
};
constexpr ReadPlan rp;
static_assert(rp.n2 >= 12 && rp.wt[31 - BI] >= 0, "");
struct QRegs { i32x4 r[8]; };
struct LV { f32x2 v[4][4]; float sc[4]; };
__device__ __forceinline__ f32x2 pair_lookup(const char* lut, int q0, int q1) { return *(const f32x2*)(lut + (q0 << 3) + (q1 << 7)); }
__device__ __forceinline__ u32x4 q_pack(const LV& l, int i) {
  u32x4 o;
  o.x = pack_bf16(l.v[i][0].x * l.sc[i], l.v[i][0].y * l.sc[i]); o.y = pack_bf16(l.v[i][1].x * l.sc[i], l.v[i][1].y * l.sc[i]);
  o.z = pack_bf16(l.v[i][2].x * l.sc[i], l.v[i][2].y * l.sc[i]); o.w = pack_bf16(l.v[i][3].x * l.sc[i], l.v[i][3].y * l.sc[i]);
  return o;
}

__global__ __launch_bounds__(512, 2) void nf4_gemm(const uint16_t* x_, const int* qw_, const uint16_t* cb_, const uint16_t* am_,
                                                   char* ws_, uint16_t* out_) {
  __shared__ __attribute__((aligned(1024))) char shm_raw[SHM_BYTES];
  char* const shm = shm_raw + HDR;
  u32x4 rsQ; u64x2 k2, k4; unsigned long long k0;
  asm volatile("s_load_dwordx2 s[20:21], %4, 8\n\ts_load_dwordx4 %1, %4, 16\n\ts_load_dwordx4 %2, %4, 32\n\ts_load_dwordx2 %3, %4, 0\n\t"
               "s_mov_b32 s22, %5\n\ts_mov_b32 s23, 0x20000"
               : "=&{s[20:23]}"(rsQ), "=&{s[8:11]}"(k2), "=&{s[16:19]}"(k4), "=&{s[4:5]}"(k0)
               : "s"(__builtin_amdgcn_kernarg_segment_ptr()), "n"(K_ * N_ * 4));
#define ARGS() asm volatile("s_waitcnt lgkmcnt(0)" : "+{s[20:23]}"(rsQ), "+{s[8:11]}"(k2), "+{s[16:19]}"(k4), "+{s[4:5]}"(k0))
#define PTRS() asm volatile("" : "+s"(k2), "+s"(k4), "+s"(k0));                                                                    \
  const char* const x = (const char*)k0; const char* const am = (const char*)k2.y; char* const ws = (char*)k4.x;                  \
  gu32* const flags = (gu32*)(ws + SYNC_OFF + (size_t)tn * FG);                                                                   \
  const __amdgpu_buffer_rsrc_t rsW = __builtin_amdgcn_make_buffer_rsrc((void*)ws, (short)0, (int)WIMG_BYTES, 0x00020000);         \
  (void)x; (void)am; (void)flags; (void)rsW
  asm volatile("s_cmp_lt_u32 %0, 32\n\ts_cbranch_scc1 .Lnf4_prefetch" :: "{s2}"(blockIdx.x), "{v0}"(threadIdx.x), "{s[4:5]}"(k0) : "scc");
  unsigned long long pc0; asm volatile("s_getpc_b64 %0" : "=s"(pc0));
  unsigned ct;
  asm volatile("global_load_dword %0, %1, %2" : "=&v"(ct) : "v"(min((unsigned)(threadIdx.x * 128u), CODE_TOUCH_MAX)), "s"(pc0) : "memory");
  const int tm = (blockIdx.x & 6) + ((blockIdx.x >> 3) & 1), tn = ((blockIdx.x >> 3) & 30) - 4 + (blockIdx.x & 1);
#define TILE_OFF(ks) (((ks) * 32 + (tn >> 1) + (tn & 1) * 16) * (2 * WB))
  const int rot = tm;
  const int tid = threadIdx.x, lane = tid & 63;
  const int wid = __builtin_amdgcn_readfirstlane(tid >> 6);
  const int lc0 = wid < 4 ? wid : wid & 1 ? 0 : 3;
#define PART(lc) (((lc) >> 1) * 1024 + ((lc) & 1) * 128)
  const int myks = wid < 4 ? rot : rot + 8;
  unsigned qvoff = (unsigned)(((lane >> 3) * 8 * N_ + (lane & 7) * 4) * 4);
  unsigned avoff = (unsigned)((myks * N_ + tn * BN) * 2 + lc0 * 64 + (lane & 7) * 8);
  unsigned evoff = (unsigned)(SYNC_OFF + tn * FG + 256);
  unsigned qso = (unsigned)(tn * BN * 4 + myks * (BK * N_ * 4) + lc0 * 128);
  unsigned co0 = (unsigned)((lane & 15) * 2), co1 = (unsigned)((tid >> 4) * 2);
  asm volatile("" : "+v"(qvoff), "+v"(avoff), "+v"(evoff), "+s"(qso), "+v"(co0), "+v"(co1));
  SCHED();
  ARGS();
  char* lut = shm + LUT_OFF;
#define Q_ROW(Q, j, so_) asm volatile("buffer_load_dwordx4 %0, %1, %2, %3 offen nt" : "=&v"(Q.r[j]) : "v"(qvoff), "s"(rsQ), "s"((so_) + (j) * (N_ * 4)) : "memory")
#define Q_ROWS(Q, so_) do { _Pragma("unroll") for (int j_ = 0; j_ < 8; j_++) Q_ROW(Q, j_, so_); } while (0)
#define Q_SC(S) asm volatile("global_load_dwordx2 %0, %1, %2" : "=&v"(S) : "v"(avoff), "s"(k2.y) : "memory")
#define TAG_LOAD(T) asm volatile("global_load_dword %0, %1, %2 sc1" : "=&v"(T) : "v"(evoff), "s"(k4.x) : "memory")
#define Q_PIN(q) asm volatile("" : "+v"(q.r[0]), "+v"(q.r[1]), "+v"(q.r[2]), "+v"(q.r[3]), "+v"(q.r[4]), "+v"(q.r[5]), "+v"(q.r[6]), "+v"(q.r[7]))
#define SCALES(S) do { asm volatile("" : "+v"(S));                                                                                \
      lv.sc[0] = __builtin_bit_cast(float, S.x << 16); lv.sc[1] = __builtin_bit_cast(float, S.x & 0xffff0000u);                   \
      lv.sc[2] = __builtin_bit_cast(float, S.y << 16); lv.sc[3] = __builtin_bit_cast(float, S.y & 0xffff0000u); } while (0)
#define ROUND(Q, dst, PUB, g_) do { Q_PIN(Q);                                                                                     \
      _Pragma("unroll") for (int i_ = 0; i_ < 4; i_++) _Pragma("unroll") for (int a_ = 0; a_ < 4; a_++)                           \
        lv.v[i_][a_] = pair_lookup(lut, Q.r[2 * a_][i_], Q.r[2 * a_ + 1][i_]);                                                    \
      SCHED();                                                                                                                    \
      char* d_ = shm + (dst) + io;                                                                                                \
      _Pragma("unroll") for (int i_ = 0; i_ < 4; i_++) { const u32x4 o_ = q_pack(lv, i_);                                         \
        if (PUB) { __builtin_amdgcn_raw_buffer_store_b128(o_, rsW, (g_) + i_ * 256, 0, AUX_SC1); SCHED(); }                       \
        *(u32x4*)(d_ + i_ * 256) = o_; }                                                                                          \
      SCHED(); } while (0)
  LV lv;

  if (__builtin_expect(wid < 4, 1)) {
    QRegs q; u32x2 qsv; unsigned c0, c1, tagv;
    asm volatile("global_load_ushort %0, %2, %4\n\tglobal_load_ushort %1, %3, %4" : "=&v"(c0), "=&v"(c1) : "v"(co0), "v"(co1), "s"(k2.x) : "memory");
    Q_ROWS(q, qso);
    Q_SC(qsv);
    TAG_LOAD(tagv);
    SCHED();
    PTRS();
    BAR();
    const int io = (lane >> 3) * KCB + (lane & 7) * 16;
    unsigned gA = (unsigned)(TILE_OFF(rot) + (PART(lc0) + io));
    asm volatile("" : "+v"(gA));
    f32x4 acc[4][8];
#pragma unroll
    for (int m = 0; m < 4; m++)
#pragma unroll
      for (int n = 0; n < 8; n++) { acc[m][n] = (f32x4){0.f, 0.f, 0.f, 0.f}; pin_acc(acc[m][n], m * 8 + n); }
    WAIT_V(10);
    asm volatile("" : "+v"(c0), "+v"(c1)); asm volatile("" :: "v"(ct));
    *(u32x2*)(lut + tid * 8) = (u32x2){c0 << 16, c1 << 16};
    WAIT_L(0);
    BAR();
    WAIT_V(0);
    SCALES(qsv);
    ROUND(q, XB + PART(lc0), 1, gA);
    BAR();
    SCHED();
    const int fr = lane & 15, fq = lane >> 4;
    const int xo0 = (wid * 64 + fr) * 128 + ((fq ^ (fr >> 1)) << 4), xo1 = xo0 ^ 64;
    const int wo = XB + fq * KCB + (fr << 4);
    bf16x8 xl[4], xh[4], wa[8], wb[8];
    typedef __attribute__((address_space(3))) const bf16x8 lds_frag;
#define LD(a_) (*(lds_frag*)(a_))
#define MF(m_, n_, X, W) do { acc[m_][n_] = __builtin_amdgcn_mfma_f32_16x16x32_bf16(X, W, acc[m_][n_], 0, 0, 0); pin_acc(acc[m_][n_], (m_) * 8 + (n_)); } while (0)
#define RW(wf, j_, W_, cb) wf[j_] = LD((W_) + ((j_) & 1) * (4 * KCB) + ((cb) * 4 + ((j_) >> 1)) * 256)
#define RX(xf, j_, A, B, off) xf[j_] = LD((((j_) & 1) ? (B) : (A)) + (off) + ((j_) >> 1) * 2048)
#define KH(i) (((i) ^ ((i) >> 1)) & 1)
#define MP(m0, cb, i, X, W) MF((m0) + (((i) >> 3) & 1), (cb) * 4 + (((i) >> 1) & 3), X[(((i) >> 2) & 2) + KH(i)], W[((i) & 6) + KH(i)])
#define VADD(d, v_, s_) asm volatile("v_add_u32 %0, %1, %2" : "=v"(d) : "s"(s_), "v"(v_))
    const unsigned lb = (unsigned)(size_t)shm;
    unsigned bW = lb + wo, bA = lb + xo0, bB = lb + xo1;
    unsigned vf = (unsigned)(((tm * BM + wid * 64 + 4 * fq) * N_ + tn * BN) * 2 + fr * 8);
    asm volatile("" : "+v"(bW), "+v"(bA), "+v"(bB), "+v"(vf));
    __amdgpu_buffer_rsrc_t rsO = __builtin_amdgcn_make_buffer_rsrc((void*)k4.y, (short)0, M_ * N_ * 2, 0x00020000);
    asm volatile("" : "+s"(rsO));
    unsigned ro[16];
#pragma unroll
    for (int j = 0; j < 16; j++) { ro[j] = (unsigned)(((j >> 2) * 16 + (j & 3)) * (N_ * 2)); asm volatile("" : "+s"(ro[j])); }
    WAIT_V(0);
    asm volatile("" : "+v"(tagv));
    if (lane == 0) __hip_atomic_store(flags + (rot * 4 + wid), TAG_OF(tagv), RLX_AGENT);
    unsigned aW = bW, aA = bA, aB = bB;
    asm volatile("" : "+v"(aW), "+v"(aA), "+v"(aB));
    unsigned so_ = 0;
    WAIT_L(0);
    SCHED();
    BAR();
#define RK(k) do { if (k == 0 || k == 2) RX(xl, (k >> 1) & 1, aA, aB, 0); else if (k == 1) RW(wa, 0, aW, 0);                       \
      else if (k < 10) RW(wa, (k - 2) & 7, aW, 0); else if (k < 12) RX(xl, (k - 8) & 3, aA, aB, 0);                               \
      else if (k < 16) RX(xh, k & 3, aA, aB, 4096); else RW(wb, k & 7, aW, 1); } while (0)
#pragma unroll
    for (int k = 0; k < rp.n2; k++) { RK(k); SCHED(); }
    asm volatile(".p2align 5\n\t.rept %0\n\ts_nop 0\n\t.endr" :: "n"(6));
    SCHED();
    WAIT_LB(rp.wt[31 - BI]);
    SCHED();
#pragma unroll 1
    for (int t = 0; t < NT - 2;) {
#pragma unroll
      for (int i = 0; i < 32; i++) {
        if (i < 16) MP(0, 0, i, xl, wa); else MP(2, 0, i, xh, wa);
        SCHED();
        wait_lb(rp.wt[32 - BI + i]);
        if (rp.rd[32 - BI + i] >= 0) RK(rp.rd[32 - BI + i]);
        SCHED();
      }
#pragma unroll
      for (int i = 0; i < 32; i++) {
        if (i == 31) { WAIT_LB(rp.wt[31 - BI]); SCHED(); }
        if (i < 16) MP(0, 1, i, xl, wb); else MP(2, 1, i, xh, wb);
        SCHED();
        if (i == 0) { so_ = so_ == 2 * SB ? 0 : so_ + SB; asm volatile("" : "+s"(so_)); }
        if (i == 3) VADD(aW, bW, so_);
        if (i == 5) VADD(aA, bA, so_);
        if (i == 7) VADD(aB, bB, so_);
        if (i == 4) { t++; asm volatile("" : "+s"(t)); }
        if (i == BI - 1) BAR();
        if (i >= BI && rp.rd[(i - BI) & 63] >= 0) RK(rp.rd[(i - BI) & 63]);
        SCHED();
      }
    }
    u32x2 ev[32];
    bf16x8 wc[8], xc[4];
#define PC_M(j) (((j) >> 4) * 2 + (((j) >> 2) & 1))
#define PC_C(j) ((((j) >> 3) ^ ((j) >> 4)) & 1)
#define PK(j, d) ev[j][d] = pack_bf16(acc[PC_M(j)][PC_C(j) * 4 + 2 * (d)][(j) & 3], acc[PC_M(j)][PC_C(j) * 4 + 2 * (d) + 1][(j) & 3])
#define OUT_ST(j) __builtin_amdgcn_raw_buffer_store_b64(ev[j], rsO, vf + PC_C(j) * 128, ro[PC_M(j) * 4 + ((j) & 3)], AUX_NT)
#define PH(p, m0, cb, XC, WC, RD) do { _Pragma("unroll") for (int i = 0; i < 16; i++) {                                           \
      MP(m0, cb, i, XC, WC); SCHED();                                                                                             \
      OUTW(p * 16 + i, RD(i)); } } while (0)
#define OUTW(u, R_) do {                                                                                                          \
      if (tl.st[u] >= 0) { OUT_ST(tl.st[u] & 31); SCHED(); }                                                                      \
      R_; SCHED();                                                                                                                \
      if (tl.pk[(u) + 1] > tl.pk[u]) PK(tl.pj[tl.pk[u]], tl.pd[tl.pk[u]]);                                                        \
      if (tl.pk[(u) + 1] > tl.pk[u] + 1) PK(tl.pj[tl.pk[u] + 1], tl.pd[tl.pk[u] + 1]);                                            \
      SCHED();                                                                                                                    \
      if (tl.st[u] >= 0) asm volatile("" :: "v"(ev[tl.st[u] & 31])); } while (0)
#define RDS(i, WN, W_, cb, XN, xA, xB, xo, x0, nx0, nx1, w14) do {                                                                \
      if (i == 5) WAIT_LB(5);                                                                                                     \
      if (i == 14) WAIT_LB(w14);                                                                                                  \
      if (i < nx0) RX(XN, (x0 + i) & 3, xA, xB, xo);                                                                              \
      if (i >= nx0 && i < nx0 + 8) RW(WN, (i - nx0) & 7, W_, cb);                                                                 \
      if (i >= nx0 + 8 && i < nx0 + nx1 + 8) RX(XN, (x0 + i - 8) & 3, xA, xB, xo); } while (0)
#define R0(i) do { if (i < 5 && rp.wt[32 - BI + i] >= 0) wait_lb(rp.wt[32 - BI + i] - rp.nf[i] + i);                              \
      RDS(i, wb, aW, 1, xh, aA, aB, 0, 0, 0, 0, 0); } while (0)
#define R1(i) RDS(i, wa, bW, 0, xh, bA, bB, 0, 0, 2, 2, 3)
#define R2(i) RDS(i, wb, bW, 1, xc, bA, bB, 4096, 0, 0, 2, 2)
#define R3(i) RDS(i, wc, aW, 1, xc, bA, bB, 4096, 2, 2, 0, 8)
#define R4(i) RDS(i, wa, aW, 0, xl, aA, aB, 4096, 0, 4, 0, 8)
#define R5(i) RDS(i, wb, bW, 0, xl, aA, aB, 0, 0, 0, 0, 0)
    PH(0, 0, 0, xl, wa, R0);
    BAR();
    SCHED();
    PH(1, 0, 1, xl, wb, R1);
    PH(2, 0, 0, xh, wa, R2);
    PH(3, 0, 1, xh, wb, R3);
    PH(4, 2, 1, xc, wb, R4);
    PH(5, 2, 1, xl, wc, R5);
#define PL(m) do { _Pragma("unroll") for (int i = 0; i < 16; i++) {                                                               \
      const int e = (m - 2) * 2 + (i & 1), f = ((i >> 1) & 6) + (i & 1);                                                          \
      if (i & 2) MF(m, i >> 2, xc[e], wb[f]); else MF(m, i >> 2, xl[e], wa[f]);                                                   \
      SCHED();                                                                                                                    \
      OUTW(64 + m * 16 + i, (void)0); } } while (0)
    PL(2);
    PL(3);
#pragma unroll
    for (int j = 28; j < 32; j++) { PK(j, 1); SCHED(); OUT_ST(j); SCHED(); }
  } else {
  PTRS();
  if (wid < 6) {
    const int h = wid - 4;
    unsigned xv[16];
#pragma unroll
    for (int i = 0; i < 16; i++) xv[i] = (unsigned)((8 * i + (lane >> 3)) * (K_ * 2) + (((lane & 7) ^ ((i & 1) * 4 + (lane >> 4))) << 4));
    const char* xbase = x + (size_t)(tm * BM + h * 128) * (K_ * 2);
    const unsigned ldsx = (unsigned)(size_t)(shm) + h * 16384;
#define X8(o, l_, b_)                                                                                                             \
      asm volatile("s_mov_b32 m0, %8\n\ts_nop 0\n\tglobal_load_lds_dwordx4 %0, %9\n\t"                                            \
                   "s_add_u32 m0, m0, 0x400\n\ts_nop 0\n\tglobal_load_lds_dwordx4 %1, %9\n\t"                                     \
                   "s_add_u32 m0, m0, 0x400\n\ts_nop 0\n\tglobal_load_lds_dwordx4 %2, %9\n\t"                                     \
                   "s_add_u32 m0, m0, 0x400\n\ts_nop 0\n\tglobal_load_lds_dwordx4 %3, %9\n\t"                                     \
                   "s_add_u32 m0, m0, 0x400\n\ts_nop 0\n\tglobal_load_lds_dwordx4 %4, %9\n\t"                                     \
                   "s_add_u32 m0, m0, 0x400\n\ts_nop 0\n\tglobal_load_lds_dwordx4 %5, %9\n\t"                                     \
                   "s_add_u32 m0, m0, 0x400\n\ts_nop 0\n\tglobal_load_lds_dwordx4 %6, %9\n\t"                                     \
                   "s_add_u32 m0, m0, 0x400\n\ts_nop 0\n\tglobal_load_lds_dwordx4 %7, %9"                                         \
                   :: "v"(xv[o]), "v"(xv[o + 1]), "v"(xv[o + 2]), "v"(xv[o + 3]), "v"(xv[o + 4]), "v"(xv[o + 5]), "v"(xv[o + 6]), "v"(xv[o + 7]), \
                      "s"(l_), "s"(b_) : "memory", "scc")
#define ISSUE_X(s, slot) do { const char* b_ = xbase + kstep_of(s, tm) * 128; const unsigned l_ = ldsx + (slot) * SB;       \
      X8(0, l_, b_); X8(8, l_ + 8192, b_); } while (0)
    BAR();
    BAR();
    BAR();
    ISSUE_X(0, 0);
    ISSUE_X(1, 1);
    WAIT_V(16);
    asm volatile("" :: "v"(ct));
    BAR();
    int xslot = 2;
#pragma unroll 1
    for (int t = -1; t < NT - 2; t++) {
      if (t + 3 < NT) { ISSUE_X(t + 3, xslot); xslot = xslot == NS - 1 ? 0 : xslot + 1; WAIT_V(16); } else { WAIT_V(0); }
      BAR();
    }
  } else {
    QRegs qa, qb; u32x2 sb, sb2; unsigned tagv;
    Q_SC(sb); avoff ^= 64; Q_SC(sb2); TAG_LOAD(tagv);
    unsigned qso2 = qso ^ 128;
    asm volatile("" : "+s"(qso2));
    const int io = (lane >> 3) * KCB + (lane & 7) * 16;
    const int fks = kstep_of(lane >> 2, tm);
    unsigned fvo = (unsigned)(fks * 16 + (fks < 8 ? (lane & 3) * 4 : 0));
    asm volatile("" : "+v"(fvo));
    BAR();
    Q_ROWS(qa, qso);
    Q_ROWS(qb, qso2);
    BAR();
    BAR();
    WAIT_V(8);
    asm volatile("" :: "v"(ct));
    SCALES(sb);
    ROUND(qa, STG_OFF + PART(lc0), 0, 0);
    WAIT_V(0);
    unsigned fv = tagv;
#define POLL(f_, m_, b_) asm volatile("s_mov_b64 exec, %3\n\tglobal_load_dword %0, %1, %2 sc1\n\ts_mov_b64 exec, -1" : "+v"(f_) : "v"(fvo), "s"(b_), "s"(m_) : "memory")
    SCALES(sb2);
    ROUND(qb, STG_OFF + PART(lc0 ^ 1), 0, 0);
    WAIT_L(0);
    asm volatile("" : "+v"(tagv));
    const unsigned tag = TAG_OF(tagv);
   if (wid == 6) {
    __builtin_amdgcn_s_setprio(3);
    const unsigned wv = (unsigned)(lane * 16), wv2 = wv + 4096, wv3 = wv + 8192, wv4 = wv + 12288;
    const unsigned ldsw = (unsigned)(size_t)(shm) + XB;
#define ISSUE_W(s, slot) do { const char* b_ = ws + (unsigned)TILE_OFF(kstep_of(s, tm)); const unsigned l_ = ldsw + (slot) * SB;  \
      asm volatile("s_mov_b32 m0, %4\n\ts_nop 0\n\t"                                                                              \
                   "global_load_lds_dwordx4 %0, %5 sc1\n\tglobal_load_lds_dwordx4 %0, %5 offset:1024 sc1\n\t"                     \
                   "global_load_lds_dwordx4 %0, %5 offset:2048 sc1\n\tglobal_load_lds_dwordx4 %0, %5 offset:3072 sc1\n\t"         \
                   "s_add_u32 m0, m0, 0x1000\n\ts_nop 0\n\t"                                                                      \
                   "global_load_lds_dwordx4 %1, %5 sc1\n\tglobal_load_lds_dwordx4 %1, %5 offset:1024 sc1\n\t"                     \
                   "global_load_lds_dwordx4 %1, %5 offset:2048 sc1\n\tglobal_load_lds_dwordx4 %1, %5 offset:3072 sc1\n\t"         \
                   "s_add_u32 m0, m0, 0x1000\n\ts_nop 0\n\t"                                                                      \
                   "global_load_lds_dwordx4 %2, %5 sc1\n\tglobal_load_lds_dwordx4 %2, %5 offset:1024 sc1\n\t"                     \
                   "global_load_lds_dwordx4 %2, %5 offset:2048 sc1\n\tglobal_load_lds_dwordx4 %2, %5 offset:3072 sc1\n\t"         \
                   "s_add_u32 m0, m0, 0x1000\n\ts_nop 0\n\t"                                                                      \
                   "global_load_lds_dwordx4 %3, %5 sc1\n\tglobal_load_lds_dwordx4 %3, %5 offset:1024 sc1\n\t"                     \
                   "global_load_lds_dwordx4 %3, %5 offset:2048 sc1\n\tglobal_load_lds_dwordx4 %3, %5 offset:3072 sc1"             \
                   :: "v"(wv), "v"(wv2), "v"(wv3), "v"(wv4), "s"(l_), "s"(b_) : "memory", "scc"); } while (0)
#define POLL_BLOCK(m_) do { unsigned f_ = ~tag; POLL(f_, m_, flags); asm volatile("s_waitcnt vmcnt(0)" : "+v"(f_) :: "memory"); rdy |= __ballot(f_ == tag); } while (0)
#define READY(s) (((unsigned)(rdy >> (4 * (s))) & 15u) == 15u)
#define NEXT_W() do { nw++; wslot = wslot == NS - 1 ? 0 : wslot + 1; } while (0)
    unsigned long long rdy = 0, want = 0xff00ull;
    unsigned spins = 0;
    BAR();
    int nw = 2, wslot = 2;
    bool preg = false;
#pragma unroll 1
    for (int t = -1; t < NT - 2; t++) {
      const int lim = t + 3 < NT ? t + 3 : NT - 1;
      if (preg) { WAIT_V(16); } else { WAIT_V(0); }
      asm volatile("" : "+v"(fv));
      rdy |= __ballot(fv == tag);
      if (nw <= t + 2) {
#pragma unroll 1
        while (!READY(nw)) { POLL_BLOCK((0xffull << (4 * nw)) & ~rdy); if (++spins > SPIN_MAX) rdy = ~0ull; }
        ISSUE_W(nw, wslot); NEXT_W();
      }
      if (t == 0) want = 0xfffffff00ull;
      if (t == POLLB_T) want = ~0xffull;
      const unsigned long long m = want & ~rdy;
      POLL(fv, m ? m : 1ull, m ? (const char*)flags : (const char*)am);
      preg = nw <= lim && READY(nw);
      if (preg) { ISSUE_W(nw, wslot); NEXT_W(); WAIT_V(17); } else { WAIT_V(1); }
      BAR();
    }
    WAIT_V(0);
    asm volatile("" : "+v"(fv));
    if (tm == 0 && lane == 0) __hip_atomic_store(flags + 64, tag, RLX_AGENT);
   } else {
    BAR();
    u32x4 bv[16];
#pragma unroll
    for (int i = 0; i < 16; i++) bv[i] = *(const u32x4*)(shm + STG_OFF + lane * 16 + i * 1024);
    WAIT_L(0);
    SCHED();
    const unsigned g_ = (unsigned)(TILE_OFF(rot + 8) + lane * 16);
#pragma unroll
    for (int t = 0; t <= 4; t++) {
      BAR();
      if (t == 1 || t == 2) {
        __builtin_amdgcn_s_sleep(10);
#pragma unroll
        for (int i = (t - 1) * 8; i < t * 8; i++) __builtin_amdgcn_raw_buffer_store_b128(bv[i], rsW, g_ + i * 1024, 0, AUX_SC1);
        SCHED();
      }
    }
    WAIT_V(0);
    asm volatile("" :: "v"(fv));
    if (lane == 0) __hip_atomic_store(flags + (rot + 8) * 4, tag, RLX_AGENT);
#pragma unroll 1
    for (int t = 5; t < NT - 1; t++) BAR();
   }
  }
  }
  asm volatile("s_endpgm\n.Lnf4_prefetch:\n\t"
               "s_and_b32 s3, s2, 6\n\ts_bfe_u32 s0, s2, 0x10003\n\ts_or_b32 s3, s3, s0\n\t"
               "v_lshlrev_b32 v2, 10, v0\n\tv_and_b32 v2, 0xfffff800, v2\n\tv_and_b32 v3, 1, v0\n\tv_lshl_or_b32 v2, v3, 6, v2\n\t"
               "v_lshl_or_b32 v2, s3, 19, v2\n\t"
               "s_mov_b32 s1, 0x9eca7538\n\ts_cmp_lt_u32 s2, 16\n\ts_cmov_b32 s1, 0xfdb16420\n\ts_mov_b32 s2, %1\n\ts_waitcnt lgkmcnt(0)\n\t"
               "s_cmp_eq_u32 s3, 3\n\ts_cbranch_scc0 3f\n\ts_sleep 1\n\ts_add_u32 s2, s2, 3\n\ts_branch 1f\n3:\n\ts_sleep 44\n"
               "1:\n\ts_xor_b32 s0, s1, s3\n\ts_and_b32 s0, s0, 15\n\t"
               "v_lshl_add_u32 v3, s0, 7, v2\n\tglobal_load_dword v4, v3, s[4:5]\n"
               "2:\n\ts_sleep %0\n\ts_sub_u32 s2, s2, 1\n\ts_cbranch_scc0 2b\n\ts_mov_b32 s2, 0\n\t"
               "s_lshr_b32 s1, s1, 4\n\ts_cbranch_scc1 1b\n\ts_waitcnt vmcnt(0)"
               :: "n"(12), "n"(2) : "memory");
}

extern "C" void kernel_launch(void* const* d_in, const int* in_sizes, int n_in, void* d_out, int out_size, void* d_ws,
                              size_t ws_size, hipStream_t stream) {
  nf4_gemm<<<256, 512, 0, stream>>>((const uint16_t*)d_in[0], (const int*)d_in[1], (const uint16_t*)d_in[2],
                                   (const uint16_t*)d_in[3], (char*)d_ws, (uint16_t*)d_out);
}
```

```cpp
#include <hip/hip_runtime.h>
#include <hip/hip_bf16.h>
#include <stdint.h>
#pragma clang diagnostic ignored "-Wint-to-pointer-cast"

typedef __attribute__((ext_vector_type(8))) short bf16x8;
typedef __attribute__((ext_vector_type(4))) float f32x4;
typedef __attribute__((ext_vector_type(4))) int i32x4;
typedef __attribute__((ext_vector_type(4))) unsigned u32x4;
typedef __attribute__((ext_vector_type(2))) unsigned u32x2;
typedef __attribute__((ext_vector_type(2))) unsigned long long u64x2;
typedef __attribute__((ext_vector_type(2))) float f32x2;
typedef __attribute__((ext_vector_type(2))) __bf16 bf16x2_t;
typedef __attribute__((address_space(1))) unsigned gu32;

constexpr int M_ = 2048, K_ = 1024, N_ = 3584;
constexpr int BM = 256, BN = 128, BK = 64, NT = K_ / BK;
constexpr int XB = BM * BK * 2;
constexpr int WB = BN * BK * 2;
constexpr int KCB = BN * 16;
constexpr int SB = XB + WB;
constexpr int NS = 3;
constexpr int HDR = WB;
constexpr int LUT_OFF = -HDR;
constexpr int STG_OFF = SB + XB;
constexpr int SHM_BYTES = HDR + NS * SB;
constexpr size_t WIMG_BYTES = (size_t)16 << 20;
constexpr size_t SYNC_OFF = WIMG_BYTES;
constexpr unsigned SPIN_MAX = 1u << 17;
constexpr int POLLB_T = 5;
constexpr int BI = 12;
#define RPLAN "r.r.rr.r.rr.r.rr.rrc" "r.D.r.I.r.r.r.rpr.r.r.r.......j."
constexpr int NU = 192, NO = 144;
constexpr int TB13 = 19, TB14 = 35, TB15 = 85;
constexpr int LASTW_T = 9;
#define UPLAN "rrWrrrWr......w." "....rrrrrrrrrrrW" "...wrrrWrrrrrrrW" "rrrWrrrW.....w.." "rr.rrr.rrr.....w" "rr.rr..........w" \
              "rr.rrr.rrr.....w" "rr.rrr.rrr.rrr.W" "rr.rrr.rrr.rrr.W" "rr.rrr.rrr.....w" "................" "................"
#define TPLAN "......1....2s122" "1.s111s111s1..s." "..s...s1..s.22s1" "11s111s111s2..s." "..s...s...s....." ".......1...111s." \
              "11s111s111s22.s." "..s...s...s.22.." "....22s.s.s.s22."
constexpr int FG = 512;
constexpr unsigned CODE_TOUCH_MAX = 12032;
constexpr int AUX_NT = 2, AUX_SC1 = 16;

#define WAIT_V(n) asm volatile("s_waitcnt vmcnt(%0)" ::"n"(n) : "memory")
#define WAIT_L(n) asm volatile("s_waitcnt lgkmcnt(%0)" ::"n"(n) : "memory")
#define WAIT_LB(n) __builtin_amdgcn_s_waitcnt(0xC07F | ((n) << 8))
#define BAR() do { asm volatile("" ::: "memory"); __builtin_amdgcn_s_barrier(); asm volatile("" ::: "memory"); } while (0)
#define SCHED() __builtin_amdgcn_sched_barrier(0)
#define TAG_OF(e) ((e) * 0x9E3779B1u + 0x7F4A7C15u)
#define RLX_AGENT __ATOMIC_RELAXED, __HIP_MEMORY_SCOPE_AGENT

__device__ __forceinline__ unsigned pack_bf16(float a, float b) {
  f32x2 v = {a, b};
  bf16x2_t r = __builtin_convertvector(v, bf16x2_t);
  return __builtin_bit_cast(unsigned, r);
}
__device__ __forceinline__ int kstep_of(int s, int tm) { return ((int)(0x9FEDCBA176543280ull >> (4 * s)) & 15) ^ tm; }

__device__ __forceinline__ void pin_acc(f32x4& a, int k) {
  switch (k) {
    case 0: asm volatile("" : "+{v[2:5]}"(a)); break;
    case 1: asm volatile("" : "+{v[6:9]}"(a)); break;
    case 2: asm volatile("" : "+{v[10:13]}"(a)); break;
    case 3: asm volatile("" : "+{v[14:17]}"(a)); break;
    case 4: asm volatile("" : "+{v[18:21]}"(a)); break;
    case 5: asm volatile("" : "+{v[22:25]}"(a)); break;
    case 6: asm volatile("" : "+{v[26:29]}"(a)); break;
    case 7: asm volatile("" : "+{v[30:33]}"(a)); break;
    case 8: asm volatile("" : "+{v[34:37]}"(a)); break;
    case 9: asm volatile("" : "+{v[38:41]}"(a)); break;
    case 10: asm volatile("" : "+{v[42:45]}"(a)); break;
    case 11: asm volatile("" : "+{v[46:49]}"(a)); break;
    case 12: asm volatile("" : "+{v[50:53]}"(a)); break;
    case 13: asm volatile("" : "+{v[54:57]}"(a)); break;
    case 14: asm volatile("" : "+{v[58:61]}"(a)); break;
    case 15: asm volatile("" : "+{v[62:65]}"(a)); break;
    case 16: asm volatile("" : "+{v[66:69]}"(a)); break;
    case 17: asm volatile("" : "+{v[70:73]}"(a)); break;
    case 18: asm volatile("" : "+{v[74:77]}"(a)); break;
    case 19: asm volatile("" : "+{v[78:81]}"(a)); break;
    case 20: asm volatile("" : "+{v[82:85]}"(a)); break;
    case 21: asm volatile("" : "+{v[86:89]}"(a)); break;
    case 22: asm volatile("" : "+{v[90:93]}"(a)); break;
    case 23: asm volatile("" : "+{v[94:97]}"(a)); break;
    case 24: asm volatile("" : "+{v[98:101]}"(a)); break;
    case 25: asm volatile("" : "+{v[102:105]}"(a)); break;
    case 26: asm volatile("" : "+{v[106:109]}"(a)); break;
    case 27: asm volatile("" : "+{v[110:113]}"(a)); break;
    case 28: asm volatile("" : "+{v[114:117]}"(a)); break;
    case 29: asm volatile("" : "+{v[118:121]}"(a)); break;
    case 30: asm volatile("" : "+{v[122:125]}"(a)); break;
    case 31: asm volatile("" : "+{v[126:129]}"(a)); break;
  }
}
__device__ __forceinline__ void wait_lb(int n) {
  switch (n) {
    case 0: WAIT_LB(0); break;   case 1: WAIT_LB(1); break;   case 2: WAIT_LB(2); break;   case 3: WAIT_LB(3); break;
    case 4: WAIT_LB(4); break;   case 5: WAIT_LB(5); break;   case 6: WAIT_LB(6); break;   case 7: WAIT_LB(7); break;
    case 8: WAIT_LB(8); break;   case 9: WAIT_LB(9); break;   case 10: WAIT_LB(10); break; case 11: WAIT_LB(11); break;
    case 12: WAIT_LB(12); break; case 13: WAIT_LB(13); break; case 14: WAIT_LB(14); break;
  }
}
struct TailPlan {
  int st[NO], pk[NO + 1], pj[64], pd[64];
  constexpr TailPlan() : st(), pk(), pj(), pd() {
    for (int k = 0; k < 64; k++) { pj[k] = k < 48 ? k >> 1 : 24 + ((k - 48) >> 3) * 4 + (k & 3); pd[k] = k < 48 ? k & 1 : (k >> 2) & 1; }
    const char* const plan =
        TPLAN;
    int k = 0, j = 0;
    for (int u = 0; u < NO; u++) {
      const char c = plan[u];
      pk[u] = k; st[u] = (c == 's' || c == 't') ? j++ : -1;
      k += c == '2' ? 2 : (c == '1' || c == 't') ? 1 : 0;
    }
    pk[NO] = k;
  }
};
constexpr TailPlan tl;
static_assert(tl.pk[NO] == 60, "");
struct ReadPlan {
  int rd[64], wt[64], n2;
  constexpr ReadPlan() : rd(), wt(), n2(0) {
    const char* const plan = RPLAN;
    int k = 0;
    for (int u = 0; u < 64 - BI; u++) {
      const char c = plan[u];
      if (u == 32 - BI) n2 = k;
      wt[u] = -1; rd[u] = -1;
      if (c != 'r' && c != '.') {
        const int i = (u - (32 - BI) + ((c | 32) - 'a' + 1)) | 1;
        const int need = i < 1 ? 2 : i < 10 ? i + 3 : i < 16 ? 12 : i < 17 ? 13 : i < 24 ? 14 : i < 25 ? 15 : i < 32 ? 16 : i < 40 ? i - 15 : 24;
        wt[u] = k - need;
      }
      if (c == 'r' || (c >= 'A' && c <= 'Z')) rd[u] = k++;
    }
    for (int u = 64 - BI; u < 64; u++) { rd[u] = -1; wt[u] = -1; }
    if (k != 24) n2 = -1;
  }
};
constexpr ReadPlan rp;
static_assert(rp.n2 == 12 && rp.wt[31 - BI] >= 0, "");
struct LastPlan {
  int m[NU], n[NU], xs[NU], xi[NU], ws[NU], wi[NU], rd[NU], wt[NU];
  int rk[128], rs[128], rj[128], rg[128], rh[128], nr;
  bool ok;
  constexpr void add(int k, int s, int j, int g, int h) { rk[nr] = k; rs[nr] = s; rj[nr] = j; rg[nr] = g; rh[nr] = h; nr++; }
  constexpr void addW(int s, int g, int h, int a = 0, int b = 8) { for (int j = a; j < b; j++) add(1, s, j ^ ((j >> 1) & 1), g, h); }
  constexpr void addX(int s, int g, int h) { for (int j = 0; j < 4; j++) add(0, s, j, g, h); }
  constexpr void addXW(int sx, int sw, int g) { add(0, sx, 0, g, 0); addW(sw, g, 0, 0, 1); add(0, sx, 1, g, 0); addW(sw, g, 0, 1, 8); add(0, sx, 2, g, 0); add(0, sx, 3, g, 0); }
  constexpr LastPlan() : m(), n(), xs(), xi(), ws(), wi(), rd(), wt(), rk(), rs(), rj(), rg(), rh(), nr(0), ok(true) {
    add(0, 0, 0, 0, 0); add(1, 0, 0, 0, 0); add(0, 0, 1, 0, 0);
    for (int j = 1; j < 8; j++) add(1, 0, j, 0, 0);
    add(0, 0, 2, 0, 0); add(0, 0, 3, 0, 0);
    addW(1, 0, 1); addXW(1, 0, 1); addXW(0, 1, 2); addW(0, 2, 1); addW(1, 1, 1); addX(0, 1, 1);
    addX(1, 2, 1); addX(2, 0, 1); addW(1, 0, 1); addW(2, 2, 0, 0, 4); addW(0, 0, 0); addW(2, 2, 0, 4, 8); addW(1, 1, 0);
    const int pg[10] = {0, 0, 1, 2, 2, 1, 1, 2, 0, 0}, pc[10] = {0, 1, 0, 0, 1, 1, 1, 1, 1, 0};
    const int px[10] = {0, 0, 1, 0, 0, 1, 0, 1, 2, 2}, pw[10] = {0, 1, 0, 1, 0, 1, 1, 0, 1, 0};
    int cur[2][3][8] = {}, need[NU] = {}, iss[NU] = {}, seen[32][6] = {};
    for (int k = 0; k < 12; k++) cur[rk[k]][rs[k]][rj[k]] = k;
    const char* const plan = UPLAN;
    int k = 12;
    for (int u = 0; u < NU; u++) {
      const int p = u >> 4, i = u & 15, kh = (i ^ (i >> 1)) & 1, e = (i >> 1) & 1, g = p < 10 ? pg[p] : 1 + e;
      if (p < 10) { m[u] = (p < 6 ? 0 : 2) + (i >> 3); n[u] = pc[p] * 4 + ((i >> 1) & 3); xs[u] = px[p]; ws[u] = pw[p]; xi[u] = ((i >> 2) & 2) + kh; wi[u] = (i & 6) + kh; }
      else { m[u] = p - 8; n[u] = i >> 2; xs[u] = e; ws[u] = 1 + e; xi[u] = (p - 10) * 2 + (i & 1); wi[u] = ((i >> 1) & 6) + (i & 1); }
      const int a = cur[0][xs[u]][xi[u]], b = cur[1][ws[u]][wi[u]];
      if (rk[a] != 0 || rg[a] != g || rh[a] * 2 + (rj[a] >> 1) != m[u] || rk[b] != 1 || rg[b] != g || rh[b] * 4 + (rj[b] >> 1) != n[u] || ((rj[a] ^ rj[b]) & 1)) ok = false;
      seen[m[u] * 8 + n[u]][g * 2 + (rj[a] & 1)]++;
      need[u] = (a > b ? a : b) + 1;
      iss[u] = k; rd[u] = -1;
      if (plan[u] == 'r' || plan[u] == 'W') {
        if (u < (rg[k] == 0 ? 0 : rg[k] == 1 ? TB13 : rk[k] == 0 && rh[k] == 1 ? TB15 : TB14)) ok = false;
        cur[rk[k]][rs[k]][rj[k]] = k; rd[u] = k++;
      }
    }
    if (k != nr) ok = false;
    for (int t = 0; t < 32; t++) for (int e = 0; e < 6; e++) if (seen[t][e] != 1) ok = false;
    for (int u = 0; u < NU; u++) {
      wt[u] = -1;
      if ((plan[u] | 32) == 'w') {
        int nd = 0;
        for (int v = u + 1; v < NU; v++) { nd = need[v] > nd ? need[v] : nd; if ((plan[v] | 32) == 'w') break; }
        if (nd > iss[u]) ok = false;
        wt[u] = iss[u] - nd > 14 ? 14 : iss[u] - nd;
      }
    }
  }
};
constexpr LastPlan lp;
static_assert(lp.ok, "");
struct QRegs { i32x4 r[8]; };
struct LV { f32x2 v[4][4]; float sc[4]; };
__device__ __forceinline__ f32x2 pair_lookup(const char* lut, int q0, int q1) { return *(const f32x2*)(lut + (q0 << 3) + (q1 << 7)); }
__device__ __forceinline__ u32x4 q_pack(const LV& l, int i) {
  u32x4 o;
  o.x = pack_bf16(l.v[i][0].x * l.sc[i], l.v[i][0].y * l.sc[i]); o.y = pack_bf16(l.v[i][1].x * l.sc[i], l.v[i][1].y * l.sc[i]);
  o.z = pack_bf16(l.v[i][2].x * l.sc[i], l.v[i][2].y * l.sc[i]); o.w = pack_bf16(l.v[i][3].x * l.sc[i], l.v[i][3].y * l.sc[i]);
  return o;
}

__global__ __launch_bounds__(512, 2) void nf4_gemm(const uint16_t* x_, const int* qw_, const uint16_t* cb_, const uint16_t* am_,
                                                   char* ws_, uint16_t* out_) {
  __shared__ __attribute__((aligned(1024))) char shm_raw[SHM_BYTES];
  char* const shm = shm_raw + HDR;
  u32x4 rsQ; u64x2 k2, k4; unsigned long long k0;
  asm volatile("s_load_dwordx2 s[20:21], %4, 8\n\ts_load_dwordx4 %1, %4, 16\n\ts_load_dwordx4 %2, %4, 32\n\ts_load_dwordx2 %3, %4, 0\n\t"
               "s_mov_b32 s22, %5\n\ts_mov_b32 s23, 0x20000"
               : "=&{s[20:23]}"(rsQ), "=&{s[8:11]}"(k2), "=&{s[16:19]}"(k4), "=&{s[4:5]}"(k0)
               : "s"(__builtin_amdgcn_kernarg_segment_ptr()), "n"(K_ * N_ * 4));
#define ARGS() asm volatile("s_waitcnt lgkmcnt(0)" : "+{s[20:23]}"(rsQ), "+{s[8:11]}"(k2), "+{s[16:19]}"(k4), "+{s[4:5]}"(k0))
#define PTRS() asm volatile("" : "+s"(k2), "+s"(k4), "+s"(k0));                                                                    \
  const char* const x = (const char*)k0; const char* const am = (const char*)k2.y; char* const ws = (char*)k4.x;                  \
  gu32* const flags = (gu32*)(ws + SYNC_OFF + (size_t)tn * FG);                                                                   \
  const __amdgpu_buffer_rsrc_t rsW = __builtin_amdgcn_make_buffer_rsrc((void*)ws, (short)0, (int)WIMG_BYTES, 0x00020000);         \
  (void)x; (void)am; (void)flags; (void)rsW
  asm volatile("s_cmpk_ge_u32 %0, 224\n\ts_cbranch_scc1 .Lnf4_prefetch" :: "{s2}"(blockIdx.x), "{v0}"(threadIdx.x), "{s[4:5]}"(k0) : "scc");
  unsigned long long pc0; asm volatile("s_getpc_b64 %0" : "=s"(pc0));
  unsigned ct;
  asm volatile("global_load_dword %0, %1, %2" : "=&v"(ct) : "v"(min((unsigned)(threadIdx.x * 128u), CODE_TOUCH_MAX)), "s"(pc0) : "memory");
  const int tm = (blockIdx.x & 6) + ((blockIdx.x >> 3) & 1), tn = ((blockIdx.x >> 3) & 30) + (blockIdx.x & 1);
#define TILE_OFF(ks) (((ks) * 32 + (tn >> 1) + (tn & 1) * 16) * (2 * WB))
  const int rot = tm;
  const int tid = threadIdx.x, lane = tid & 63;
  const int wid = __builtin_amdgcn_readfirstlane(tid >> 6);
  const int lc0 = wid < 4 ? wid : wid & 1 ? 0 : 3;
#define PART(lc) (((lc) >> 1) * 1024 + ((lc) & 1) * 128)
  const int myks = wid < 4 ? rot : rot + 8;
  unsigned qvoff = (unsigned)(((lane >> 3) * 8 * N_ + (lane & 7) * 4) * 4);
  unsigned avoff = (unsigned)((myks * N_ + tn * BN) * 2 + lc0 * 64 + (lane & 7) * 8);
  unsigned evoff = (unsigned)(SYNC_OFF + tn * FG + 256);
  unsigned qso = (unsigned)(tn * BN * 4 + myks * (BK * N_ * 4) + lc0 * 128);
  unsigned co0 = (unsigned)((lane & 15) * 2), co1 = (unsigned)((tid >> 4) * 2);
  asm volatile("" : "+v"(qvoff), "+v"(avoff), "+v"(evoff), "+s"(qso), "+v"(co0), "+v"(co1));
  SCHED();
  ARGS();
  char* lut = shm + LUT_OFF;
#define Q_ROW(Q, j, so_) asm volatile("buffer_load_dwordx4 %0, %1, %2, %3 offen nt" : "=&v"(Q.r[j]) : "v"(qvoff), "s"(rsQ), "s"((so_) + (j) * (N_ * 4)) : "memory")
#define Q_ROWS(Q, so_) do { _Pragma("unroll") for (int j_ = 0; j_ < 8; j_++) Q_ROW(Q, j_, so_); } while (0)
#define Q_SC(S) asm volatile("global_load_dwordx2 %0, %1, %2" : "=&v"(S) : "v"(avoff), "s"(k2.y) : "memory")
#define TAG_LOAD(T) asm volatile("global_load_dword %0, %1, %2 sc1" : "=&v"(T) : "v"(evoff), "s"(k4.x) : "memory")
#define Q_PIN(q) asm volatile("" : "+v"(q.r[0]), "+v"(q.r[1]), "+v"(q.r[2]), "+v"(q.r[3]), "+v"(q.r[4]), "+v"(q.r[5]), "+v"(q.r[6]), "+v"(q.r[7]))
#define SCALES(S) do { asm volatile("" : "+v"(S));                                                                                \
      lv.sc[0] = __builtin_bit_cast(float, S.x << 16); lv.sc[1] = __builtin_bit_cast(float, S.x & 0xffff0000u);                   \
      lv.sc[2] = __builtin_bit_cast(float, S.y << 16); lv.sc[3] = __builtin_bit_cast(float, S.y & 0xffff0000u); } while (0)
#define ROUND(Q, dst, PUB, g_) do { Q_PIN(Q);                                                                                     \
      _Pragma("unroll") for (int i_ = 0; i_ < 4; i_++) _Pragma("unroll") for (int a_ = 0; a_ < 4; a_++)                           \
        lv.v[i_][a_] = pair_lookup(lut, Q.r[2 * a_][i_], Q.r[2 * a_ + 1][i_]);                                                    \
      SCHED();                                                                                                                    \
      char* d_ = shm + (dst) + io;                                                                                                \
      _Pragma("unroll") for (int i_ = 0; i_ < 4; i_++) { const u32x4 o_ = q_pack(lv, i_);                                         \
        if (PUB) { __builtin_amdgcn_raw_buffer_store_b128(o_, rsW, (g_) + i_ * 256, 0, AUX_SC1); SCHED(); }                       \
        *(u32x4*)(d_ + i_ * 256) = o_; }                                                                                          \
      SCHED(); } while (0)
  LV lv;

  if (__builtin_expect(wid < 4, 1)) {
    QRegs q; u32x2 qsv; unsigned c0, c1, tagv;
    asm volatile("global_load_ushort %0, %2, %4\n\tglobal_load_ushort %1, %3, %4" : "=&v"(c0), "=&v"(c1) : "v"(co0), "v"(co1), "s"(k2.x) : "memory");
    Q_ROWS(q, qso);
    Q_SC(qsv);
    TAG_LOAD(tagv);
    SCHED();
    PTRS();
    BAR();
    const int io = (lane >> 3) * KCB + (lane & 7) * 16;
    unsigned gA = (unsigned)(TILE_OFF(rot) + (PART(lc0) + io));
    asm volatile("" : "+v"(gA));
    f32x4 acc[4][8];
#pragma unroll
    for (int m = 0; m < 4; m++)
#pragma unroll
      for (int n = 0; n < 8; n++) { acc[m][n] = (f32x4){0.f, 0.f, 0.f, 0.f}; pin_acc(acc[m][n], m * 8 + n); }
    WAIT_V(10);
    asm volatile("" : "+v"(c0), "+v"(c1)); asm volatile("" :: "v"(ct));
    *(u32x2*)(lut + tid * 8) = (u32x2){c0 << 16, c1 << 16};
    WAIT_L(0);
    BAR();
    WAIT_V(0);
    SCALES(qsv);
    ROUND(q, XB + PART(lc0), 1, gA);
    BAR();
    SCHED();
    const int fr = lane & 15, fq = lane >> 4;
    const int xo0 = (wid * 64 + fr) * 128 + ((fq ^ (fr >> 1)) << 4), xo1 = xo0 ^ 64;
    const int wo = XB + fq * KCB + (fr << 4);
    bf16x8 xl[4], xh[4], wa[8], wb[8];
    typedef __attribute__((address_space(3))) const bf16x8 lds_frag;
#define LD(a_) (*(lds_frag*)(a_))
#define MF(m_, n_, X, W) do { acc[m_][n_] = __builtin_amdgcn_mfma_f32_16x16x32_bf16(X, W, acc[m_][n_], 0, 0, 0); pin_acc(acc[m_][n_], (m_) * 8 + (n_)); } while (0)
#define RW(wf, j_, W_, cb) wf[j_] = LD((W_) + ((j_) & 1) * (4 * KCB) + ((cb) * 4 + ((j_) >> 1)) * 256)
#define RX(xf, j_, A, B, off) xf[j_] = LD((((j_) & 1) ? (B) : (A)) + (off) + ((j_) >> 1) * 2048)
#define KH(i) (((i) ^ ((i) >> 1)) & 1)
#define MP(m0, cb, i, X, W) MF((m0) + (((i) >> 3) & 1), (cb) * 4 + (((i) >> 1) & 3), X[(((i) >> 2) & 2) + KH(i)], W[((i) & 6) + KH(i)])
#define VADD(d, v_, s_) asm volatile("v_add_u32 %0, %1, %2" : "=v"(d) : "s"(s_), "v"(v_))
    const unsigned lb = (unsigned)(size_t)shm;
    unsigned bW = lb + wo, bA = lb + xo0, bB = lb + xo1;
    unsigned vf = (unsigned)(((tm * BM + wid * 64 + 4 * fq) * N_ + tn * BN) * 2 + fr * 8);
    asm volatile("" : "+v"(bW), "+v"(bA), "+v"(bB), "+v"(vf));
    __amdgpu_buffer_rsrc_t rsO = __builtin_amdgcn_make_buffer_rsrc((void*)k4.y, (short)0, M_ * N_ * 2, 0x00020000);
    asm volatile("" : "+s"(rsO));
    unsigned ro[16];
#pragma unroll
    for (int j = 0; j < 16; j++) { ro[j] = (unsigned)(((j >> 2) * 16 + (j & 3)) * (N_ * 2)); asm volatile("" : "+s"(ro[j])); }
    WAIT_V(0);
    asm volatile("" : "+v"(tagv));
    if (lane == 0) __hip_atomic_store(flags + (rot * 4 + wid), TAG_OF(tagv), RLX_AGENT);
    unsigned eW = bW - SB;
    asm volatile("" : "+v"(eW));
    unsigned aW = bW, aA = bA, aB = bB;
    asm volatile("" : "+v"(aW), "+v"(aA), "+v"(aB));
    unsigned so_ = 0;
    WAIT_L(0);
    SCHED();
    BAR();
#define RK(k) do { if (k == 0 || k == 2) RX(xl, (k >> 1) & 1, aA, aB, 0); else if (k == 1) RW(wa, 0, aW, 0);                       \
      else if (k < 10) RW(wa, (k - 2) & 7, aW, 0); else if (k < 12) RX(xl, (k - 8) & 3, aA, aB, 0);                               \
      else if (k < 16) RX(xh, k & 3, aA, aB, 4096); else RW(wb, k & 7, aW, 1); } while (0)
#pragma unroll
    for (int k = 0; k < rp.n2; k++) { RK(k); SCHED(); }
    asm volatile(".p2align 5\n\t.rept %0\n\ts_nop 0\n\t.endr" :: "n"(6));
    SCHED();
    WAIT_LB(rp.wt[31 - BI]);
    SCHED();
#pragma unroll 1
    for (int t = 0; t < NT - 3;) {
#pragma unroll
      for (int i = 0; i < 32; i++) {
        if (i < 16) MP(0, 0, i, xl, wa); else MP(2, 0, i, xh, wa);
        SCHED();
        wait_lb(rp.wt[32 - BI + i]);
        if (rp.rd[32 - BI + i] >= 0) RK(rp.rd[32 - BI + i]);
        SCHED();
      }
#pragma unroll
      for (int i = 0; i < 32; i++) {
        if (i == 31) { WAIT_LB(rp.wt[31 - BI]); SCHED(); }
        if (i < 16) MP(0, 1, i, xl, wb); else MP(2, 1, i, xh, wb);
        SCHED();
        if (i == 0) { so_ = so_ == 2 * SB ? 0 : so_ + SB; asm volatile("" : "+s"(so_)); }
        if (i == 3) VADD(aW, bW, so_);
        if (i == 5) VADD(aA, bA, so_);
        if (i == 7) VADD(aB, bB, so_);
        if (i == 4) { t++; asm volatile("" : "+s"(t)); }
        if (i == BI - 1) BAR();
        if (i >= BI && rp.rd[(i - BI) & 63] >= 0) RK(rp.rd[(i - BI) & 63]);
        SCHED();
      }
    }
    u32x2 ev[32];
#define PC_M(j) (((j) >> 4) * 2 + (((j) >> 2) & 1))
#define PC_C(j) ((((j) >> 3) ^ ((j) >> 4)) & 1)
#define PK(j, d) ev[j][d] = pack_bf16(acc[PC_M(j)][PC_C(j) * 4 + 2 * (d)][(j) & 3], acc[PC_M(j)][PC_C(j) * 4 + 2 * (d) + 1][(j) & 3])
#define OUT_ST(j) __builtin_amdgcn_raw_buffer_store_b64(ev[j], rsO, vf + PC_C(j) * 128, ro[PC_M(j) * 4 + ((j) & 3)], AUX_NT)
#define OUTW(u, R_) do {                                                                                                          \
      if (tl.st[u] >= 0) { OUT_ST(tl.st[u] & 31); SCHED(); }                                                                      \
      R_; SCHED();                                                                                                                \
      if (tl.pk[(u) + 1] > tl.pk[u]) PK(tl.pj[tl.pk[u]], tl.pd[tl.pk[u]]);                                                        \
      if (tl.pk[(u) + 1] > tl.pk[u] + 1) PK(tl.pj[tl.pk[u] + 1], tl.pd[tl.pk[u] + 1]);                                            \
      SCHED();                                                                                                                    \
      if (tl.st[u] >= 0) asm volatile("" :: "v"(ev[tl.st[u] & 31])); } while (0)
    bf16x8 xt[3][4], wt[3][8];
#pragma unroll
    for (int j = 0; j < 4; j++) xt[0][j] = xl[j];
#pragma unroll
    for (int j = 0; j < 8; j++) wt[0][j] = wa[j];
#define LRD(k) do { const int j_ = lp.rj[k], o_ = lp.rg[k] == 1 ? SB : 0;                                                          \
      if (lp.rk[k]) RW(wt[lp.rs[k]], j_, (lp.rg[k] == 2 ? eW : aW) + o_, lp.rh[k]);                                               \
      else RX(xt[lp.rs[k]], j_ & 3, lp.rg[k] == 2 ? bA : aA, lp.rg[k] == 2 ? bB : aB, o_ + lp.rh[k] * 4096); } while (0)
#define LRW(u) do { wait_lb(lp.wt[u]); if (lp.rd[u] >= 0) LRD(lp.rd[u] & 127); } while (0)
#define PH(p) do { _Pragma("unroll") for (int i = 0; i < 16; i++) { const int u = p * 16 + i;                                     \
      MF(lp.m[u], lp.n[u], xt[lp.xs[u]][lp.xi[u]], wt[lp.ws[u]][lp.wi[u]]); SCHED();                                              \
      if (u == TB13 || u == TB14 || u == TB15) { BAR(); SCHED(); }                                                                \
      if (u < NU - NO) { LRW(u); SCHED(); } else OUTW(u < NU - NO ? 0 : u - (NU - NO), LRW(u)); } } while (0)
    PH(0); PH(1); PH(2); PH(3); PH(4); PH(5); PH(6); PH(7); PH(8); PH(9); PH(10); PH(11);
#pragma unroll
    for (int j = 28; j < 32; j++) { PK(j, 1); SCHED(); OUT_ST(j); SCHED(); }
  } else {
  PTRS();
  if (wid < 6) {
    const int h = wid - 4;
    unsigned xv[16];
#pragma unroll
    for (int i = 0; i < 16; i++)
      xv[i] = (unsigned)((8 * ((i & 3) + (i & 4) * 2 + (i & 8) / 2) + (lane >> 3)) * (K_ * 2) + (((lane & 7) ^ ((i & 1) * 4 + (lane >> 4))) << 4));
    const char* xbase = x + (size_t)(tm * BM + h * 128) * (K_ * 2);
    const unsigned ldsx = (unsigned)(size_t)(shm) + h * 16384;
#define X8(o, l_, b_)                                                                                                             \
      asm volatile("s_mov_b32 m0, %8\n\ts_nop 0\n\tglobal_load_lds_dwordx4 %0, %9\n\t"                                            \
                   "s_add_u32 m0, m0, 0x400\n\ts_nop 0\n\tglobal_load_lds_dwordx4 %1, %9\n\t"                                     \
                   "s_add_u32 m0, m0, 0x400\n\ts_nop 0\n\tglobal_load_lds_dwordx4 %2, %9\n\t"                                     \
                   "s_add_u32 m0, m0, 0x400\n\ts_nop 0\n\tglobal_load_lds_dwordx4 %3, %9\n\t"                                     \
                   "s_add_u32 m0, m0, 0x1400\n\ts_nop 0\n\tglobal_load_lds_dwordx4 %4, %9\n\t"                                    \
                   "s_add_u32 m0, m0, 0x400\n\ts_nop 0\n\tglobal_load_lds_dwordx4 %5, %9\n\t"                                     \
                   "s_add_u32 m0, m0, 0x400\n\ts_nop 0\n\tglobal_load_lds_dwordx4 %6, %9\n\t"                                     \
                   "s_add_u32 m0, m0, 0x400\n\ts_nop 0\n\tglobal_load_lds_dwordx4 %7, %9"                                         \
                   :: "v"(xv[o]), "v"(xv[o + 1]), "v"(xv[o + 2]), "v"(xv[o + 3]), "v"(xv[o + 4]), "v"(xv[o + 5]), "v"(xv[o + 6]), "v"(xv[o + 7]), \
                      "s"(l_), "s"(b_) : "memory", "scc")
#define ISSUE_X(s, slot) do { const char* b_ = xbase + kstep_of(s, tm) * 128; const unsigned l_ = ldsx + (slot) * SB;       \
      X8(0, l_, b_); X8(8, l_ + 4096, b_); } while (0)
    BAR();
    BAR();
    BAR();
    ISSUE_X(0, 0);
    ISSUE_X(1, 1);
    WAIT_V(16);
    asm volatile("" :: "v"(ct));
    BAR();
    int xslot = 2;
#pragma unroll 1
    for (int t = -1; t < NT - 4; t++) {
      ISSUE_X(t + 3, xslot); xslot = xslot == NS - 1 ? 0 : xslot + 1; WAIT_V(16);
      BAR();
    }
    const char* const last = xbase + kstep_of(NT - 1, tm) * 128;
    X8(0, ldsx, last); WAIT_V(8);
    BAR();
    X8(8, ldsx + 4096, last); WAIT_V(8);
    BAR();
    WAIT_V(0);
    BAR();
  } else {
    QRegs qa, qb; u32x2 sb, sb2; unsigned tagv;
    Q_SC(sb); avoff ^= 64; Q_SC(sb2); TAG_LOAD(tagv);
    unsigned qso2 = qso ^ 128;
    asm volatile("" : "+s"(qso2));
    const int io = (lane >> 3) * KCB + (lane & 7) * 16;
    const int fks = kstep_of(lane >> 2, tm);
    unsigned fvo = (unsigned)(fks * 16 + (fks < 8 ? (lane & 3) * 4 : 0));
    asm volatile("" : "+v"(fvo));
    BAR();
    Q_ROWS(qa, qso);
    Q_ROWS(qb, qso2);
    BAR();
    BAR();
    WAIT_V(8);
    asm volatile("" :: "v"(ct));
    SCALES(sb);
    ROUND(qa, STG_OFF + PART(lc0), 0, 0);
    WAIT_V(0);
    unsigned fv = tagv;
#define POLL(f_, m_, b_) asm volatile("s_mov_b64 exec, %3\n\tglobal_load_dword %0, %1, %2 sc1\n\ts_mov_b64 exec, -1" : "+v"(f_) : "v"(fvo), "s"(b_), "s"(m_) : "memory")
    SCALES(sb2);
    ROUND(qb, STG_OFF + PART(lc0 ^ 1), 0, 0);
    WAIT_L(0);
    asm volatile("" : "+v"(tagv));
    const unsigned tag = TAG_OF(tagv);
    const unsigned wv = (unsigned)(lane * 16), wv2 = wv + 4096, wv3 = wv + 8192, wv4 = wv + 12288;
    const unsigned ldsw = (unsigned)(size_t)(shm) + XB;
#define ISSUE_W(s, slot) do { const char* b_ = ws + (unsigned)TILE_OFF(kstep_of(s, tm)); const unsigned l_ = ldsw + (slot) * SB;  \
      asm volatile("s_mov_b32 m0, %4\n\ts_nop 0\n\t"                                                                              \
                   "global_load_lds_dwordx4 %0, %5 sc1\n\tglobal_load_lds_dwordx4 %0, %5 offset:1024 sc1\n\t"                     \
                   "global_load_lds_dwordx4 %0, %5 offset:2048 sc1\n\tglobal_load_lds_dwordx4 %0, %5 offset:3072 sc1\n\t"         \
                   "s_add_u32 m0, m0, 0x1000\n\ts_nop 0\n\t"                                                                      \
                   "global_load_lds_dwordx4 %1, %5 sc1\n\tglobal_load_lds_dwordx4 %1, %5 offset:1024 sc1\n\t"                     \
                   "global_load_lds_dwordx4 %1, %5 offset:2048 sc1\n\tglobal_load_lds_dwordx4 %1, %5 offset:3072 sc1\n\t"         \
                   "s_add_u32 m0, m0, 0x1000\n\ts_nop 0\n\t"                                                                      \
                   "global_load_lds_dwordx4 %2, %5 sc1\n\tglobal_load_lds_dwordx4 %2, %5 offset:1024 sc1\n\t"                     \
                   "global_load_lds_dwordx4 %2, %5 offset:2048 sc1\n\tglobal_load_lds_dwordx4 %2, %5 offset:3072 sc1\n\t"         \
                   "s_add_u32 m0, m0, 0x1000\n\ts_nop 0\n\t"                                                                      \
                   "global_load_lds_dwordx4 %3, %5 sc1\n\tglobal_load_lds_dwordx4 %3, %5 offset:1024 sc1\n\t"                     \
                   "global_load_lds_dwordx4 %3, %5 offset:2048 sc1\n\tglobal_load_lds_dwordx4 %3, %5 offset:3072 sc1"             \
                   :: "v"(wv), "v"(wv2), "v"(wv3), "v"(wv4), "s"(l_), "s"(b_) : "memory", "scc"); } while (0)
#define POLL_BLOCK(m_) do { unsigned f_ = ~tag; POLL(f_, m_, flags); asm volatile("s_waitcnt vmcnt(0)" : "+v"(f_) :: "memory"); rdy |= __ballot(f_ == tag); } while (0)
   if (wid == 6) {
    __builtin_amdgcn_s_setprio(3);
#define READY(s) (((unsigned)(rdy >> (4 * (s))) & 15u) == 15u)
#define NEXT_W() do { nw++; wslot = wslot == NS - 1 ? 0 : wslot + 1; } while (0)
    unsigned long long rdy = 0, want = 0xff00ull;
    unsigned spins = 0;
    BAR();
    int nw = 2, wslot = 2;
    bool preg = false;
#pragma unroll 1
    for (int t = -1; t < NT - 3; t++) {
      const int lim = t + 3 < NT - 1 ? t + 3 : NT - 2;
      if (preg) { WAIT_V(16); } else { WAIT_V(0); }
      asm volatile("" : "+v"(fv));
      rdy |= __ballot(fv == tag);
      if (nw <= t + 2) {
#pragma unroll 1
        while (!READY(nw)) { POLL_BLOCK((0xffull << (4 * nw)) & ~rdy); if (++spins > SPIN_MAX) rdy = ~0ull; }
        ISSUE_W(nw, wslot); NEXT_W();
      }
      if (t == 0) want = 0xfffffff00ull;
      if (t == POLLB_T) want = (~0ull >> 4) & ~0xffull;
      const unsigned long long m = want & ~rdy;
      POLL(fv, m ? m : 1ull, m ? (const char*)flags : (const char*)am);
      preg = nw <= lim && READY(nw);
      if (preg) { ISSUE_W(nw, wslot); NEXT_W(); WAIT_V(17); } else { WAIT_V(1); }
      BAR();
    }
    BAR();
    BAR();
    WAIT_V(0);
    asm volatile("" : "+v"(fv));
    if (tm == 0 && lane == 0) __hip_atomic_store(flags + 64, tag, RLX_AGENT);
   } else {
    BAR();
    u32x4 bv[16];
#pragma unroll
    for (int i = 0; i < 16; i++) bv[i] = *(const u32x4*)(shm + STG_OFF + lane * 16 + i * 1024);
    WAIT_L(0);
    SCHED();
    const unsigned g_ = (unsigned)(TILE_OFF(rot + 8) + lane * 16);
#pragma unroll
    for (int t = 0; t <= 4; t++) {
      BAR();
      if (t == 1 || t == 2) {
        __builtin_amdgcn_s_sleep(10);
#pragma unroll
        for (int i = (t - 1) * 8; i < t * 8; i++) __builtin_amdgcn_raw_buffer_store_b128(bv[i], rsW, g_ + i * 1024, 0, AUX_SC1);
        SCHED();
      }
    }
    WAIT_V(0);
    asm volatile("" :: "v"(fv));
    if (lane == 0) __hip_atomic_store(flags + (rot + 8) * 4, tag, RLX_AGENT);
#pragma unroll 1
    for (int t = 5; t < LASTW_T; t++) BAR();
    POLL(fv, 1ull << 60, flags);
    BAR();
    asm volatile("s_waitcnt vmcnt(0)" : "+v"(fv) :: "memory");
    unsigned long long rdy = __ballot(fv == tag);
    if (rdy) { __builtin_amdgcn_s_sleep(10); ISSUE_W(NT - 1, -1); }
#pragma unroll 1
    for (int t = LASTW_T + 1; t < NT - 3; t++) BAR();
    if (!rdy) {
      unsigned spins = 0;
#pragma unroll 1
      while (!rdy) { POLL_BLOCK(1ull << 60); if (++spins > SPIN_MAX) rdy = 1; }
      ISSUE_W(NT - 1, -1);
    }
    WAIT_V(0);
    BAR(); BAR(); BAR();
   }
  }
  }
  asm volatile("s_endpgm\n.Lnf4_prefetch:\n\t"
               "s_and_b32 s3, s2, 6\n\ts_bfe_u32 s0, s2, 0x10003\n\ts_or_b32 s3, s3, s0\n\t"
               "v_lshlrev_b32 v2, 10, v0\n\tv_and_b32 v2, 0xfffff800, v2\n\tv_and_b32 v3, 1, v0\n\tv_lshl_or_b32 v2, v3, 6, v2\n\t"
               "v_lshl_or_b32 v2, s3, 19, v2\n\t"
               "s_mov_b32 s1, 0x9eca7538\n\ts_cmpk_lt_u32 s2, 240\n\ts_cmov_b32 s1, 0xfdb16420\n\ts_mov_b32 s2, %1\n\ts_waitcnt lgkmcnt(0)\n\t"
               "s_cmp_eq_u32 s3, 3\n\ts_cbranch_scc0 3f\n\ts_sleep 1\n\ts_add_u32 s2, s2, 3\n\ts_branch 1f\n3:\n\ts_sleep 44\n"
               "1:\n\ts_xor_b32 s0, s1, s3\n\ts_and_b32 s0, s0, 15\n\t"
               "v_lshl_add_u32 v3, s0, 7, v2\n\tglobal_load_dword v4, v3, s[4:5]\n"
               "2:\n\ts_sleep %0\n\ts_sub_u32 s2, s2, 1\n\ts_cbranch_scc0 2b\n\ts_mov_b32 s2, 0\n\t"
               "s_lshr_b32 s1, s1, 4\n\ts_cbranch_scc1 1b\n\ts_waitcnt vmcnt(0)"
               :: "n"(12), "n"(2) : "memory");
}

extern "C" void kernel_launch(void* const* d_in, const int* in_sizes, int n_in, void* d_out, int out_size, void* d_ws,
                              size_t ws_size, hipStream_t stream) {
  nf4_gemm<<<256, 512, 0, stream>>>((const uint16_t*)d_in[0], (const int*)d_in[1], (const uint16_t*)d_in[2],
                                   (const uint16_t*)d_in[3], (char*)d_ws, (uint16_t*)d_out);
}
```
